```python
import torch

def _softmax(x, axis=-1):
    return torch.softmax(x, dim=axis)
def _gelu(x):
    return torch.nn.functional.gelu(x, approximate="tanh")
def _silu(x):
    return torch.nn.functional.silu(x)
def _sigmoid(x):
    return torch.sigmoid(x)
def _layernorm(x, axis=-1, eps=1e-5):
    m = x.mean(dim=axis, keepdim=True)
    v = x.var(dim=axis, keepdim=True, unbiased=False)
    return (x - m) / torch.sqrt(v + eps)
def _scatter_add(tgt, idx, src, axis=0):
    return tgt.clone().index_add_(axis, idx, src)
def _segment_sum(src, seg, n):
    out = torch.zeros((n,) + src.shape[1:], dtype=src.dtype)
    return out.index_add_(0, seg, src)
def _pad(x, pw):
    flat = tuple(v for pair in reversed(pw) for v in pair)
    return torch.nn.functional.pad(x, flat)

kernel_name = 'parallel_sum__sliding_block_cached__geglu_ffn__decode'

def setup_inputs():

    _w = (lambda a: (lambda t: t.to(torch.bfloat16) if t.is_floating_point() else t)(torch.as_tensor(a)))
    def _A_setup():
        B, Sq, D = 2, 1, 512
        Hq, Hkv, Sk, Dh = 32, 32, 4096, 128
        assert Hq % Hkv == 0, (Hq, Hkv)
        return {
            "x": _w((lambda *s: torch.randn(*s, dtype=torch.bfloat16) * 0.05)(B, Sq, D)),
            "k_cache": _w((lambda *s: torch.randn(*s, dtype=torch.bfloat16) * 3.2)(B, Hkv, Sk, Dh)),
            "v_cache": _w((lambda *s: torch.randn(*s, dtype=torch.bfloat16) * 1.6)(B, Hkv, Sk, Dh)),
            "wq": _w((lambda *s: torch.randn(*s, dtype=torch.bfloat16) * 3.2)(D, Hq * Dh) * (D ** -0.5)),
            "wo": _w((lambda *s: torch.randn(*s, dtype=torch.bfloat16) * 1.6)(Hq * Dh, D) * ((Hq * Dh) ** -0.5)),
        }
    _A_in = _A_setup()

    def _B_setup():
        B, S, D, F = 2, 1, 512, 1376
        return {
            "x": _w((lambda *s: torch.randn(*s, dtype=torch.bfloat16) * 0.05)(B, S, D)),
            "w_gate": _w((lambda *s: torch.randn(*s, dtype=torch.bfloat16) * 0.05)(D, F)),
            "w_up": _w((lambda *s: torch.randn(*s, dtype=torch.bfloat16) * 0.05)(D, F)),
            "w_down": _w((lambda *s: torch.randn(*s, dtype=torch.bfloat16) * 0.05)(F, D)),
        }
    _B_in = _B_setup()

    return {
        "A_x": _A_in["x"],
        "A_k_cache": _A_in["k_cache"],
        "A_v_cache": _A_in["v_cache"],
        "A_wq": _A_in["wq"],
        "A_wo": _A_in["wo"],
        "B_x": _B_in["x"],
        "B_w_gate": _B_in["w_gate"],
        "B_w_up": _B_in["w_up"],
        "B_w_down": _B_in["w_down"],
    }

def reference(A_x, A_k_cache, A_v_cache, A_wq, A_wo, B_x, B_w_gate, B_w_up, B_w_down):
    def _A_ref(x, k_cache, v_cache, wq, wo):
        B, Sq, D = x.shape
        Hq, Dh = 32, 128
        Hkv, Sk = k_cache.shape[1], k_cache.shape[2]
        W = min(4096, max(256, Sk // 4))
        rep = Hq // Hkv
        q = (lambda x, *d: x.permute(*d))((x @ wq).reshape(B, Sq, Hq, Dh), 0, 2, 1, 3)
        k = (lambda x, n, axis: torch.repeat_interleave(x, n, dim=axis))(k_cache[:, :, Sk - W:, :], rep, axis=1)
        v = (lambda x, n, axis: torch.repeat_interleave(x, n, dim=axis))(v_cache[:, :, Sk - W:, :], rep, axis=1)
        scores = torch.einsum("bhqd,bhkd->bhqk", q, k) * (Dh ** -0.5)
        p = _softmax(scores, axis=-1)
        o = torch.einsum("bhqk,bhkd->bhqd", p, v)
        return (lambda x, *d: x.permute(*d))(o, 0, 2, 1, 3).reshape(B, Sq, Hq * Dh) @ wo

    def _B_ref(x, w_gate, w_up, w_down):
        return (_gelu(x @ w_gate) * (x @ w_up)) @ w_down

    x = A_x
    xn = _layernorm(x, axis=-1)
    return x + _A_ref(xn, A_k_cache, A_v_cache, A_wq, A_wo) + _B_ref(xn, B_w_gate, B_w_up, B_w_down)
```

```cpp
#include <hip/hip_runtime.h>
#include <stdint.h>

__global__ void __launch_bounds__(640, 3) fused_decode(const void* x, void* ws, const void* wq, const void* wg, const void* wu, const void* kc,
                                                       const void* vc, const void* wo, const void* wd, void* out) {
  __shared__ __attribute__((aligned(16))) char lds[163840];
  unsigned tid = threadIdx.x;
  asm volatile(
    "s_memrealtime s[30:31]\n"
    "s_load_dwordx16 s[4:19], s[0:1], 0x0\n"
    "s_load_dwordx4 s[20:23], s[0:1], 0x40\n"
    "v_readfirstlane_b32 s29, v0\n"
    "s_and_b32 s24, s2, 7\n"
    "s_lshr_b32 s25, s2, 6\n"
    "s_lshl2_add_u32 s24, s24, s25\n"
    "s_bfe_u32 s25, s2, 0x30003\n"
    "s_and_b32 s26, s25, 1\n"
    "s_lshr_b32 s27, s25, 1\n"
    "s_mul_i32 s28, s24, 172\n"
    "s_lshr_b32 s28, s28, 5\n"
    "v_and_b32 v0, 63, v0\n"
    "v_mov_b32 v5, 162304\n"
    "s_mov_b32 s40, 4194304\n"
    "s_lshr_b32 s29, s29, 6\n"
    "s_sub_u32 s29, s29, 1\n"
    "s_cbranch_scc1 .Lfd_loader\n"
    "s_barrier\n"
    "s_branch .Lfd_others\n"
    ".Lfd_loader:\n"
    "s_setprio 2\n"
    "v_and_b32 v1, 7, v0\n"
    "v_lshl_add_u32 v2, v1, 2, v5\n"
    "v_mov_b32 v3, 0\n"
    "ds_write_b32 v2, v3\n"
    "s_getpc_b64 s[98:99]\n"
    ".Lfd_pc:\n"
    "s_mul_i32 s34, s25, 1280\n"
    "s_addk_i32 s34, 384\n"
    "v_lshl_add_u32 v2, v0, 7, s34\n"
    "v_min_u32 v2, .Lfd_end-.Lfd_pc-4, v2\n"
    "s_mov_b32 m0, 162368\n"
    "s_mov_b64 exec, 0x3ff\n"
    "global_load_lds_dword v2, s[98:99]\n"
    "s_mov_b64 exec, -1\n"
    "v_lshlrev_b32 v4, 4, v0\n"
    "v_lshrrev_b32 v2, 3, v0\n"
    "v_bfe_u32 v3, v0, 4, 1\n"
    "v_lshlrev_b32 v3, 1, v3\n"
    "v_xor_b32 v3, v3, v1\n"
    "v_lshlrev_b32 v17, 4, v3\n"
    "v_lshl_add_u32 v6, v2, 13, v17\n"
    "v_xor_b32 v7, 64, v6\n"
    "v_add_u32 v7, 64512, v7\n"
    "v_add_u32 v8, 129024, v6\n"
    "v_add_u32 v9, 129024, v7\n"
    "v_lshlrev_b32 v3, 2, v0\n"
    "s_lshl_b32 s34, s27, 20\n"
    "s_lshl_b32 s35, s24, 8\n"
    "s_or_b32 s34, s34, s35\n"
    "s_lshl_b32 s35, s26, 7\n"
    "s_or_b32 s34, s34, s35\n"
    "s_mov_b32 m0, 126976\n"
    "s_waitcnt lgkmcnt(0)\n"
    "global_load_lds_dwordx4 v4, s[4:5]\n"
    "global_load_lds_dwordx4 v4, s[4:5] offset:1024\n"
    "s_mov_b32 m0, 162336\n"
    "s_mov_b64 exec, 3\n"
    "global_load_lds_dword v3, s[6:7]\n"
    "s_mov_b64 exec, -1\n"
    "s_add_u32 s34, s8, s34\n"
    "s_addc_u32 s35, s9, 0\n"
    "s_mov_b32 m0, 131072\n"
    "s_nop 0\n"
    "global_load_lds_dwordx4 v6, s[34:35] offset:0 nt\n"
    "global_load_lds_dwordx4 v7, s[34:35] offset:1024 nt\n"
    "global_load_lds_dwordx4 v8, s[34:35] offset:2048 nt\n"
    "global_load_lds_dwordx4 v9, s[34:35] offset:3072 nt\n"
    "s_movk_i32 s36, 5504\n"
    "v_mad_u32_u24 v10, v2, s36, v17\n"
    "v_xor_b32 v11, 64, v17\n"
    "v_mad_u32_u24 v11, v2, s36, v11\n"
    "v_add_u32 v11, 43008, v11\n"
    "v_add_u32 v12, 86016, v10\n"
    "v_add_u32 v13, 86016, v11\n"
    "s_barrier\n"
    "s_lshr_b32 s55, s2, 1\n"
    "s_and_b32 s58, s2, 1\n"
    "s_mul_i32 s56, s58, 96\n"
    "s_mov_b32 s57, 3\n"
    "s_cmp_lt_u32 s2, 168\n"
    "s_cbranch_scc1 .Lfd_gb1\n"
    "s_sub_u32 s55, s55, 42\n"
    "s_lshl_b32 s56, s58, 7\n"
    "s_mov_b32 s57, 4\n"
    "s_cmp_ge_u32 s2, 252\n"
    "s_cbranch_scc1 .Lfd_gb1\n"
    "s_sub_u32 s55, s2, 168\n"
    "s_movk_i32 s56, 192\n"
    "s_mov_b32 s57, 2\n"
    "s_mov_b32 s58, 2\n"
    ".Lfd_gb1:\n"
    "s_mov_b32 m0, 135168\n"
    "s_add_u32 s34, s34, 0x40000\n"
    "s_addc_u32 s35, s35, 0\n"
    "global_load_lds_dwordx4 v6, s[34:35] offset:0 nt\n"
    "global_load_lds_dwordx4 v7, s[34:35] offset:1024 nt\n"
    "global_load_lds_dwordx4 v8, s[34:35] offset:2048 nt\n"
    "global_load_lds_dwordx4 v9, s[34:35] offset:3072 nt\n"
    "s_mov_b32 m0, 139264\n"
    "s_add_u32 s34, s34, 0x40000\n"
    "s_addc_u32 s35, s35, 0\n"
    "global_load_lds_dwordx4 v6, s[34:35] offset:0 nt\n"
    "global_load_lds_dwordx4 v7, s[34:35] offset:1024 nt\n"
    "global_load_lds_dwordx4 v8, s[34:35] offset:2048 nt\n"
    "global_load_lds_dwordx4 v9, s[34:35] offset:3072 nt\n"
    "s_mov_b32 m0, 143360\n"
    "s_add_u32 s34, s34, 0x40000\n"
    "s_addc_u32 s35, s35, 0\n"
    "global_load_lds_dwordx4 v6, s[34:35] offset:0 nt\n"
    "global_load_lds_dwordx4 v7, s[34:35] offset:1024 nt\n"
    "global_load_lds_dwordx4 v8, s[34:35] offset:2048 nt\n"
    "global_load_lds_dwordx4 v9, s[34:35] offset:3072 nt\n"
    "s_cmp_lt_u32 s55, 43\n"
    "s_cselect_b32 s34, s10, s12\n"
    "s_cselect_b32 s35, s11, s13\n"
    "s_cselect_b32 s36, 0, 43\n"
    "s_sub_u32 s36, s55, s36\n"
    "s_mul_i32 s37, s56, 43\n"
    "s_add_u32 s36, s36, s37\n"
    "s_lshl_b32 s36, s36, 7\n"
    "s_add_u32 s34, s34, s36\n"
    "s_addc_u32 s35, s35, 0\n"
    "s_mov_b32 s45, 106496\n"
    "s_mov_b32 s36, s57\n"
    ".Lfd_gu:\n"
    "s_mov_b32 m0, s45\n"
    "s_add_u32 s45, s45, 4096\n"
    "global_load_lds_dwordx4 v10, s[34:35] offset:0 nt\n"
    "global_load_lds_dwordx4 v11, s[34:35] offset:1024 nt\n"
    "global_load_lds_dwordx4 v12, s[34:35] offset:2048 nt\n"
    "global_load_lds_dwordx4 v13, s[34:35] offset:3072 nt\n"
    "s_add_u32 s34, s34, 176128\n"
    "s_addc_u32 s35, s35, 0\n"
    "s_sub_u32 s36, s36, 1\n"
    "s_cmp_lg_u32 s36, 0\n"
    "s_cbranch_scc1 .Lfd_gu\n"
    "v_lshrrev_b32 v1, 4, v0\n"
    "v_and_b32 v3, 15, v0\n"
    "v_and_b32 v18, 1, v1\n"
    "v_lshlrev_b32 v18, 8, v18\n"
    "v_lshrrev_b32 v19, 1, v1\n"
    "v_lshl_add_u32 v18, v19, 9, v18\n"
    "v_xor_b32 v4, v3, v1\n"
    "v_lshl_add_u32 v6, v4, 4, v18\n"
    "v_xor_b32 v7, 64, v6\n"
    "v_xor_b32 v8, 128, v6\n"
    "v_xor_b32 v9, 192, v6\n"
    "v_add_u32 v6, 4096, v6\n"
    "v_add_u32 v7, 35840, v7\n"
    "v_add_u32 v8, 67584, v8\n"
    "v_add_u32 v9, 99328, v9\n"
    "v_lshlrev_b32 v4, 1, v1\n"
    "v_xor_b32 v4, v4, v3\n"
    "v_lshl_add_u32 v10, v4, 4, v18\n"
    "v_xor_b32 v12, 128, v10\n"
    "v_add_u32 v11, 35840, v10\n"
    "v_add_u32 v10, 4096, v10\n"
    "v_add_u32 v13, 99328, v12\n"
    "v_add_u32 v12, 67584, v12\n"
    "v_lshl_add_u32 v14, v2, 10, v17\n"
    "v_xor_b32 v15, 64, v14\n"
    "v_add_u32 v15, 7168, v15\n"
    "v_add_u32 v16, 14336, v14\n"
    "v_add_u32 v17, 14336, v15\n"
    "s_lshl_b32 s34, s26, 5\n"
    "s_add_u32 s34, s34, s24\n"
    "s_lshl_b32 s34, s34, 20\n"
    "s_and_b32 s35, s27, 1\n"
    "s_lshl_b32 s35, s35, 13\n"
    "s_add_u32 s34, s34, s35\n"
    "s_lshr_b32 s35, s27, 1\n"
    "s_lshl_b32 s35, s35, 14\n"
    "s_add_u32 s34, s34, s35\n"
    "s_add_u32 s34, s34, 782336\n"
    "s_add_u32 s14, s14, s34\n"
    "s_addc_u32 s15, s15, 0\n"
    "s_add_u32 s16, s16, s34\n"
    "s_addc_u32 s17, s17, 0\n"
    "s_lshl1_add_u32 s42, s24, s26\n"
    "s_nop 0\n"
    "s_movk_i32 s43, 32\n"
    "s_movk_i32 s44, 0\n"
    "s_mov_b32 s45, 0\n"
    "s_movk_i32 s46, 232\n"
    "v_mov_b32 v1, 1\n"
    "s_cmp_eq_u32 s57, 3\n"
    "s_cbranch_scc1 .Lfd_x3\n"
    "s_cmp_eq_u32 s57, 4\n"
    "s_cbranch_scc1 .Lfd_x4\n"
    "s_waitcnt vmcnt(24)\n"
    ".Lfd_x3:\n"
    "s_waitcnt vmcnt(28)\n"
    ".Lfd_x4:\n"
    "s_waitcnt vmcnt(32)\n"
    "ds_write_b32 v5, v1 offset:24\n"
    "s_memrealtime s[36:37]\n"
    "s_waitcnt lgkmcnt(0)\n"
    "s_sub_u32 s39, s36, s30\n"
    "s_add_u32 s39, s39, 46\n"
    "s_lshl_b32 s39, s39, 4\n"
    "s_max_i32 s44, s44, s39\n"
    ".Lfd_kv:\n"
    "s_bfe_u32 s34, s42, 0x10000\n"
    "s_lshl_b32 s34, s34, 12\n"
    "s_bfe_u32 s35, s42, 0x10001\n"
    "s_lshl_b32 s35, s35, 17\n"
    "s_or_b32 s34, s34, s35\n"
    "s_bfe_u32 s35, s42, 0x20002\n"
    "s_lshl_b32 s35, s35, 10\n"
    "s_or_b32 s34, s34, s35\n"
    "s_add_u32 s34, s14, s34\n"
    "s_addc_u32 s35, s15, 0\n"
    "s_mov_b32 m0, s45\n"
    ".Lfd_pc2:\n"
    "s_memrealtime s[36:37]\n"
    "s_getreg_b32 s38, hwreg(HW_REG_IB_STS)\n"
    "s_bfe_u32 s39, s38, 0x20016\n"
    "s_and_b32 s38, s38, 15\n"
    "s_lshl4_add_u32 s38, s39, s38\n"
    "s_sub_u32 s39, s43, s38\n"
    "v_mov_b32 v1, s39\n"
    "ds_write_b32 v5, v1\n"
    "s_cmp_gt_u32 s38, 40\n"
    "s_waitcnt lgkmcnt(0)\n"
    "s_cbranch_scc1 .Lfd_pc2\n"
    "s_sub_u32 s39, s36, s30\n"
    "s_lshl_b32 s39, s39, 4\n"
    "s_cmp_lt_i32 s39, s44\n"
    "s_cbranch_scc1 .Lfd_pc2\n"
    "global_load_lds_dwordx4 v6, s[34:35] offset:0 nt\n"
    "global_load_lds_dwordx4 v7, s[34:35] offset:1024 nt\n"
    "global_load_lds_dwordx4 v8, s[34:35] offset:2048 nt\n"
    "global_load_lds_dwordx4 v9, s[34:35] offset:3072 nt\n"
    "s_add_u32 s42, s42, 1\n"
    "s_add_u32 s43, s43, 4\n"
    "s_add_u32 s45, s45, 4096\n"
    "s_add_u32 s44, s44, s46\n"
    "s_cmp_lg_u32 s43, 96\n"
    "s_cbranch_scc1 .Lfd_kv1\n"
    "v_mov_b32 v6, v10\n"
    "v_mov_b32 v7, v11\n"
    "v_mov_b32 v8, v12\n"
    "v_mov_b32 v9, v13\n"
    "s_mov_b64 s[14:15], s[16:17]\n"
    ".Lfd_kv1:\n"
    "s_cmp_ge_u32 s43, 160\n"
    "s_cbranch_scc1 .Lfd_w\n"
    "s_cmp_lg_u32 s43, 136\n"
    "s_cbranch_scc1 .Lfd_kv\n"
    ".Lfd_w3:\n"
    "ds_read_b32 v1, v5 offset:8\n"
    "s_waitcnt lgkmcnt(0)\n"
    "v_readfirstlane_b32 s38, v1\n"
    "s_cmp_ge_u32 s38, 9\n"
    "s_cbranch_scc1 .Lfd_d4\n"
    "s_sleep 1\n"
    "s_sub_i32 s40, s40, 1\n"
    "s_cmp_gt_i32 s40, 0\n"
    "s_cbranch_scc1 .Lfd_w3\n"
    ".Lfd_d4:\n"
    "s_branch .Lfd_kv\n"
    ".Lfd_w:\n"
    "s_lshl_b32 s34, s28, 13\n"
    "s_lshl_b32 s35, s25, 7\n"
    "s_add_u32 s34, s34, s35\n"
    "s_add_u32 s20, s20, s34\n"
    "s_addc_u32 s21, s21, 0\n"
    "s_lshl_b32 s34, s24, 17\n"
    "s_add_u32 s34, s34, s35\n"
    "s_add_u32 s18, s18, s34\n"
    "s_addc_u32 s19, s19, 0\n"
    "s_add_u32 s44, s44, 320\n"
    "s_mov_b32 s45, 131072\n"
    "s_mov_b32 m0, 147456\n"
    ".Lfd_pc5:\n"
    "s_memrealtime s[36:37]\n"
    "s_getreg_b32 s38, hwreg(HW_REG_IB_STS)\n"
    "s_bfe_u32 s39, s38, 0x20016\n"
    "s_and_b32 s38, s38, 15\n"
    "s_lshl4_add_u32 s38, s39, s38\n"
    "s_sub_u32 s39, s43, s38\n"
    "v_mov_b32 v1, s39\n"
    "ds_write_b32 v5, v1\n"
    "s_cmp_gt_u32 s38, 40\n"
    "s_waitcnt lgkmcnt(0)\n"
    "s_cbranch_scc1 .Lfd_pc5\n"
    "s_sub_u32 s39, s36, s30\n"
    "s_lshl_b32 s39, s39, 4\n"
    "s_cmp_lt_i32 s39, s44\n"
    "s_cbranch_scc1 .Lfd_pc5\n"
    "global_load_lds_dwordx4 v14, s[20:21] offset:0 nt\n"
    "global_load_lds_dwordx4 v15, s[20:21] offset:1024 nt\n"
    "global_load_lds_dwordx4 v16, s[20:21] offset:2048 nt\n"
    "global_load_lds_dwordx4 v17, s[20:21] offset:3072 nt\n"
    "s_add_u32 s20, s20, 32768\n"
    "s_addc_u32 s21, s21, 0\n"
    "s_mov_b32 m0, 151552\n"
    "s_add_u32 s43, s43, 6\n"
    "s_add_u32 s44, s44, 348\n"
    "global_load_lds_dwordx4 v14, s[20:21] offset:0 nt\n"
    "global_load_lds_dwordx4 v15, s[20:21] offset:1024 nt\n"
    "v_mov_b32 v6, v14\n"
    "v_add_u32 v7, 31744, v14\n"
    "v_add_u32 v8, 63488, v14\n"
    "v_add_u32 v9, 95232, v14\n"
    ".Lfd_wo:\n"
    "s_mov_b32 m0, s45\n"
    ".Lfd_pc6:\n"
    "s_memrealtime s[36:37]\n"
    "s_getreg_b32 s38, hwreg(HW_REG_IB_STS)\n"
    "s_bfe_u32 s39, s38, 0x20016\n"
    "s_and_b32 s38, s38, 15\n"
    "s_lshl4_add_u32 s38, s39, s38\n"
    "s_sub_u32 s39, s43, s38\n"
    "v_mov_b32 v1, s39\n"
    "ds_write_b32 v5, v1\n"
    "s_cmp_gt_u32 s38, 40\n"
    "s_waitcnt lgkmcnt(0)\n"
    "s_cbranch_scc1 .Lfd_pc6\n"
    "s_sub_u32 s39, s36, s30\n"
    "s_lshl_b32 s39, s39, 4\n"
    "s_cmp_lt_i32 s39, s44\n"
    "s_cbranch_scc1 .Lfd_pc6\n"
    "global_load_lds_dwordx4 v6, s[18:19] offset:0 nt\n"
    "global_load_lds_dwordx4 v7, s[18:19] offset:1024 nt\n"
    "global_load_lds_dwordx4 v8, s[18:19] offset:2048 nt\n"
    "global_load_lds_dwordx4 v9, s[18:19] offset:3072 nt\n"
    "v_xor_b32 v6, 64, v6\n"
    "v_xor_b32 v7, 64, v7\n"
    "v_xor_b32 v8, 64, v8\n"
    "v_xor_b32 v9, 64, v9\n"
    "s_add_u32 s18, s18, 8192\n"
    "s_addc_u32 s19, s19, 0\n"
    "s_add_u32 s43, s43, 4\n"
    "s_add_u32 s44, s44, 232\n"
    "s_add_u32 s45, s45, 4096\n"
    "s_cmp_lt_u32 s43, 182\n"
    "s_cbranch_scc1 .Lfd_wo\n"
    "s_mov_b64 exec, 1\n"
    ".Lfd_pc7:\n"
    "s_getreg_b32 s38, hwreg(HW_REG_IB_STS)\n"
    "s_bfe_u32 s39, s38, 0x20016\n"
    "s_and_b32 s38, s38, 15\n"
    "s_lshl4_add_u32 s38, s39, s38\n"
    "s_sub_u32 s39, s43, s38\n"
    "v_mov_b32 v1, s39\n"
    "ds_write_b32 v5, v1\n"
    "s_cmp_gt_u32 s38, 0\n"
    "s_waitcnt lgkmcnt(0)\n"
    "s_cbranch_scc1 .Lfd_pc7\n"
    "s_mov_b64 exec, -1\n"
    "s_endpgm\n"
    ".Lfd_others:\n"
    "v_mov_b32 v26, 1\n"
    "v_lshrrev_b32 v1, 4, v0\n"
    "v_and_b32 v2, 15, v0\n"
    "s_cmp_eq_u32 s29, 8\n"
    "s_cbranch_scc1 .Lfd_aux\n"
    "v_bfe_u32 v3, v0, 2, 2\n"
    "v_and_b32 v4, 3, v0\n"
    "s_mov_b32 s44, 0x000f000f\n"
    "s_mov_b32 s45, s44\n"
    "s_mov_b32 s46, 0x00030003\n"
    "s_mov_b32 s47, s46\n"
    "s_mov_b32 s48, 0x11111111\n"
    "s_mov_b32 s49, s48\n"
    "s_mov_b32 s50, 0xcccccccc\n"
    "s_mov_b32 s51, s50\n"
    "s_mov_b32 s52, 0xaaaaaaaa\n"
    "s_mov_b32 s53, s52\n"
    "s_mov_b32 s41, 0\n"
    "v_lshl_add_u32 v28, v1, 3, v3\n"
    "v_lshlrev_b32 v29, 3, v4\n"
    "v_and_b32 v30, 1, v0\n"
    "v_bfe_u32 v31, v0, 1, 1\n"
    "v_and_b32 v32, 1, v1\n"
    "v_lshrrev_b32 v33, 1, v3\n"
    "v_lshlrev_b32 v13, 4, v1\n"
    "v_add_u32 v13, 159744, v13\n"
    "v_lshl_add_u32 v21, v30, 8, v13\n"
    "v_lshl_add_u32 v22, v30, 7, v13\n"
    "v_lshlrev_b32 v23, 3, v1\n"
    "v_lshl_add_u32 v23, v30, 7, v23\n"
    "v_add_u32 v23, 160832, v23\n"
    "s_and_b32 s98, s29, 3\n"
    "v_lshl_or_b32 v34, v32, 1, v33\n"
    "v_xor_b32 v34, s98, v34\n"
    "v_lshlrev_b32 v34, 5, v34\n"
    "v_lshl_add_u32 v24, v28, 7, v29\n"
    "v_add3_u32 v24, v24, v34, v26\n"
    "v_add_u32 v24, 131071, v24\n"
    "v_and_b32 v34, 2, v1\n"
    "v_or_b32 v34, v34, v33\n"
    "v_xor_b32 v34, s29, v34\n"
    "v_lshlrev_b32 v34, 5, v34\n"
    "v_lshl_add_u32 v25, v1, 2, v3\n"
    "v_lshl_add_u32 v25, v25, 7, v29\n"
    "v_add_u32 v25, v25, v34\n"
    "v_add_u32 v25, 151552, v25\n"
    "v_lshl_or_b32 v34, v32, 2, v3\n"
    "v_xor_b32 v34, s29, v34\n"
    "v_lshlrev_b32 v34, 5, v34\n"
    "v_lshl_add_u32 v16, v28, 8, v29\n"
    "v_add_u32 v16, v16, v34\n"
    "v_add_u32 v16, 65536, v16\n"
    "s_lshl_b32 s98, s29, 13\n"
    "v_xor_b32 v34, v1, v2\n"
    "v_lshlrev_b32 v34, 4, v34\n"
    "v_lshl_add_u32 v9, v2, 8, v34\n"
    "v_add_u32 v9, s98, v9\n"
    "v_xor_b32 v10, 64, v9\n"
    "v_xor_b32 v11, 128, v9\n"
    "v_xor_b32 v12, 192, v9\n"
    "s_lshl_b32 s98, s29, 6\n"
    "s_add_u32 s98, s98, 161280\n"
    "v_lshlrev_b32 v34, 3, v1\n"
    "v_lshl_add_u32 v14, v2, 5, v34\n"
    "v_add_u32 v14, s98, v14\n"
    "v_lshlrev_b32 v15, 3, v0\n"
    "v_add_u32 v15, 159744, v15\n"
    "s_lshl_b32 s99, s24, 3\n"
    "s_lshl2_add_u32 s99, s26, s99\n"
    "s_add_u32 s99, s99, s27\n"
    "s_lshl_b32 s98, s99, 10\n"
    "s_lshl_b32 s34, s29, 7\n"
    "s_add_u32 s98, s98, s34\n"
    "s_add_u32 s98, s98, 589824\n"
    "v_lshl_add_u32 v34, v1, 1, v2\n"
    "v_lshl_add_u32 v17, v34, 4, s98\n"
    "s_lshr_b32 s34, s29, 2\n"
    "s_lshl_b32 s35, s24, 1\n"
    "s_add_u32 s35, s35, s34\n"
    "s_and_b32 s36, s29, 3\n"
    "v_lshrrev_b32 v34, 2, v0\n"
    "s_lshl_b32 s37, s35, 12\n"
    "s_lshl_b32 s38, s36, 8\n"
    "s_add_u32 s37, s37, s38\n"
    "s_add_u32 s37, s37, 589824\n"
    "v_lshl_add_u32 v18, v34, 4, s37\n"
    "v_lshl_add_u32 v18, v4, 10, v18\n"
    "s_lshl_b32 s37, s35, 6\n"
    "s_add_u32 s37, s37, 4096\n"
    "v_lshl_add_u32 v19, v4, 4, s37\n"
    "s_lshl_b32 s37, s34, 8\n"
    "s_lshl_b32 s38, s36, 6\n"
    "s_add_u32 s37, s37, s38\n"
    "s_add_u32 s37, s37, 160256\n"
    "v_lshl_add_u32 v20, v34, 2, s37\n"
    "s_lshl2_add_u32 s37, s25, s29\n"
    "s_lshl_b32 s37, s37, 13\n"
    "s_lshl_b32 s38, s24, 7\n"
    "s_add_u32 s37, s37, s38\n"
    "s_add_u32 s37, s37, 851968\n"
    "v_lshl_add_u32 v34, v1, 1, v31\n"
    "v_lshl_add_u32 v6, v34, 4, s37\n"
    "v_lshl_add_u32 v6, v30, 12, v6\n"
    "s_lshr_b32 s55, s2, 1\n"
    "s_and_b32 s58, s2, 1\n"
    "s_mul_i32 s56, s58, 96\n"
    "s_mov_b32 s57, 3\n"
    "s_cmp_lt_u32 s2, 168\n"
    "s_cbranch_scc1 .Lfd_gb8\n"
    "s_sub_u32 s55, s55, 42\n"
    "s_lshl_b32 s56, s58, 7\n"
    "s_mov_b32 s57, 4\n"
    "s_cmp_ge_u32 s2, 252\n"
    "s_cbranch_scc1 .Lfd_gb8\n"
    "s_sub_u32 s55, s2, 168\n"
    "s_movk_i32 s56, 192\n"
    "s_mov_b32 s57, 2\n"
    "s_mov_b32 s58, 2\n"
    ".Lfd_gb8:\n"
    "s_lshl_b32 s59, s57, 2\n"
    "s_sub_u32 s59, 32, s59\n"
    "s_cmp_ge_u32 s29, 4\n"
    "s_cbranch_scc1 .Lfd_pgu\n"
    "s_lshl_b32 s37, s24, 13\n"
    "s_lshl_b32 s38, s27, 10\n"
    "s_add_u32 s37, s37, s38\n"
    "s_lshl_b32 s38, s26, 9\n"
    "s_add_u32 s37, s37, s38\n"
    "s_lshl_b32 s38, s29, 7\n"
    "s_add_u32 s37, s37, s38\n"
    "s_add_u32 s37, s37, 65536\n"
    "v_lshl_add_u32 v8, v34, 4, s37\n"
    "v_lshl_add_u32 v8, v30, 12, v8\n"
    "v_mul_u32_u24 v7, 3072, v1\n"
    "v_add_u32 v7, v7, v24\n"
    "s_branch .Lfd_pdone\n"
    ".Lfd_pgu:\n"
    "s_cmp_ge_u32 s55, 43\n"
    "s_cselect_b32 s36, 43, 0\n"
    "s_cselect_b32 s37, 6, 0\n"
    "s_sub_u32 s36, s55, s36\n"
    "s_lshl2_add_u32 s36, s36, s29\n"
    "s_sub_u32 s36, s36, 4\n"
    "s_cmp_ge_u32 s36, 86\n"
    "s_cselect_b32 s38, 86, 0\n"
    "s_cselect_b32 s60, 3, 0\n"
    "s_cselect_b32 s61, 1024, 0\n"
    "s_sub_u32 s36, s36, s38\n"
    "s_add_u32 s37, s37, s60\n"
    "s_add_u32 s37, s37, s58\n"
    "s_mul_i32 s37, s37, 11008\n"
    "s_lshl_b32 s36, s36, 6\n"
    "s_add_u32 s37, s37, s36\n"
    "s_add_u32 s37, s37, 327680\n"
    "v_mul_u32_u24 v8, 5504, v30\n"
    "v_lshl_add_u32 v8, v1, 4, v8\n"
    "v_add_u32 v8, s37, v8\n"
    "s_lshl1_add_u32 s61, s56, s61\n"
    "s_add_u32 s61, s61, 122880\n"
    "v_lshlrev_b32 v7, 4, v1\n"
    "v_lshl_add_u32 v7, v30, 9, v7\n"
    "v_add_u32 v7, s61, v7\n"
    "v_add_u32 v24, 4294942720, v24\n"
    ".Lfd_pdone:\n"
    "s_lshl_b32 s99, s99, 4\n"
    "s_add_u32 s99, s99, 4096\n"
    "s_waitcnt lgkmcnt(0)\n"
    ".Lfd_w9:\n"
    "ds_read_b32 v27, v5 offset:24\n"
    "s_waitcnt lgkmcnt(0)\n"
    "v_readfirstlane_b32 s38, v27\n"
    "s_cmp_ge_u32 s38, 1\n"
    "s_cbranch_scc1 .Lfd_d10\n"
    "s_sub_i32 s40, s40, 1\n"
    "s_cmp_gt_i32 s40, 0\n"
    "s_cbranch_scc1 .Lfd_w9\n"
    ".Lfd_d10:\n"
    "ds_read_b64 v[62:63], v5 offset:32\n"
    "s_waitcnt lgkmcnt(0)\n"
    "v_readfirstlane_b32 s42, v62\n"
    "v_readfirstlane_b32 s43, v63\n"
    "s_add_u32 s42, s42, 0x7f4a7c15\n"
    "s_addc_u32 s43, s43, 0x9e3779b9\n"
    "v_mov_b32 v61, s42\n"
    "v_mov_b32 v63, s43\n"
    ".Lfd_w11:\n"
    "ds_read_b32 v27, v5 offset:16\n"
    "s_waitcnt lgkmcnt(0)\n"
    "v_readfirstlane_b32 s38, v27\n"
    "s_cmp_ge_u32 s38, 1\n"
    "s_cbranch_scc1 .Lfd_d12\n"
    "s_sleep 1\n"
    "s_sub_i32 s40, s40, 1\n"
    "s_cmp_gt_i32 s40, 0\n"
    "s_cbranch_scc1 .Lfd_w11\n"
    ".Lfd_d12:\n"
    "s_cmp_ge_u32 s29, 4\n"
    "s_cbranch_scc1 .Lfd_p1g\n"
    "ds_read_b128 v[28:31], v21 offset:0\n"
    "ds_read_b128 v[32:35], v21 offset:64\n"
    "ds_read_b128 v[36:39], v21 offset:128\n"
    "ds_read_b128 v[40:43], v21 offset:192\n"
    ".Lfd_w13:\n"
    "ds_read_b32 v27, v5 offset:0\n"
    "s_waitcnt lgkmcnt(0)\n"
    "v_readfirstlane_b32 s41, v27\n"
    "s_cmp_ge_u32 s41, s59\n"
    "s_cbranch_scc1 .Lfd_d14\n"
    "s_sleep 1\n"
    "s_sub_i32 s40, s40, 1\n"
    "s_cmp_gt_i32 s40, 0\n"
    "s_cbranch_scc1 .Lfd_w13\n"
    ".Lfd_d14:\n"
    "ds_read_b64_tr_b16 v[44:45], v24 offset:0\n"
    "ds_read_b64_tr_b16 v[46:47], v24 offset:512\n"
    "ds_read_b64_tr_b16 v[48:49], v24 offset:4096\n"
    "ds_read_b64_tr_b16 v[50:51], v24 offset:4608\n"
    "ds_read_b64_tr_b16 v[52:53], v24 offset:8192\n"
    "ds_read_b64_tr_b16 v[54:55], v24 offset:8704\n"
    "ds_read_b64_tr_b16 v[56:57], v24 offset:12288\n"
    "ds_read_b64_tr_b16 v[58:59], v24 offset:12800\n"
    "s_mov_b64 exec, 1\n"
    "ds_add_u32 v5, v26 offset:8\n"
    "s_mov_b64 exec, -1\n"
    "s_waitcnt lgkmcnt(7)\n"
    "v_mfma_f32_16x16x32_bf16 v[88:91], v[44:47], v[28:31], 0\n"
    "s_waitcnt lgkmcnt(5)\n"
    "v_mfma_f32_16x16x32_bf16 v[88:91], v[48:51], v[32:35], v[88:91]\n"
    "s_waitcnt lgkmcnt(3)\n"
    "v_mfma_f32_16x16x32_bf16 v[88:91], v[52:55], v[36:39], v[88:91]\n"
    "s_waitcnt lgkmcnt(1)\n"
    "v_mfma_f32_16x16x32_bf16 v[88:91], v[56:59], v[40:43], v[88:91]\n"
    "s_nop 7\n"
    "v_cndmask_b32 v60, v88, v90, s[50:51]\n"
    "v_cndmask_b32 v62, v89, v91, s[50:51]\n"
    "s_mov_b64 exec, s[44:45]\n"
    "s_branch .Lfd_p1s\n"
    ".Lfd_p1g:\n"
    "ds_read_b128 v[28:31], v7 offset:0\n"
    "ds_read_b128 v[32:35], v7 offset:64\n"
    "ds_read_b128 v[36:39], v7 offset:128\n"
    "ds_read_b128 v[40:43], v7 offset:192\n"
    ".Lfd_w15:\n"
    "ds_read_b32 v27, v5 offset:0\n"
    "s_waitcnt lgkmcnt(0)\n"
    "v_readfirstlane_b32 s41, v27\n"
    "s_cmp_ge_u32 s41, 32\n"
    "s_cbranch_scc1 .Lfd_d16\n"
    "s_sleep 1\n"
    "s_sub_i32 s40, s40, 1\n"
    "s_cmp_gt_i32 s40, 0\n"
    "s_cbranch_scc1 .Lfd_w15\n"
    ".Lfd_d16:\n"
    "ds_read_b64_tr_b16 v[44:45], v24 offset:0\n"
    "ds_read_b64_tr_b16 v[46:47], v24 offset:512\n"
    "ds_read_b64_tr_b16 v[48:49], v24 offset:4096\n"
    "ds_read_b64_tr_b16 v[50:51], v24 offset:4608\n"
    "ds_read_b64_tr_b16 v[52:53], v24 offset:8192\n"
    "ds_read_b64_tr_b16 v[54:55], v24 offset:8704\n"
    "ds_read_b64_tr_b16 v[56:57], v24 offset:12288\n"
    "ds_read_b64_tr_b16 v[58:59], v24 offset:12800\n"
    "s_mov_b64 exec, 1\n"
    "ds_add_u32 v5, v26 offset:8\n"
    "s_mov_b64 exec, -1\n"
    "s_waitcnt lgkmcnt(7)\n"
    "v_mfma_f32_16x16x32_bf16 v[88:91], v[44:47], v[28:31], 0\n"
    "s_waitcnt lgkmcnt(5)\n"
    "v_mfma_f32_16x16x32_bf16 v[88:91], v[48:51], v[32:35], v[88:91]\n"
    "s_cmp_le_u32 s57, 2\n"
    "s_cbranch_scc1 .Lfd_p1e\n"
    "s_waitcnt lgkmcnt(3)\n"
    "v_mfma_f32_16x16x32_bf16 v[88:91], v[52:55], v[36:39], v[88:91]\n"
    "s_cmp_le_u32 s57, 3\n"
    "s_cbranch_scc1 .Lfd_p1e\n"
    "s_waitcnt lgkmcnt(1)\n"
    "v_mfma_f32_16x16x32_bf16 v[88:91], v[56:59], v[40:43], v[88:91]\n"
    "s_cmp_eq_u32 s58, 0\n"
    "s_cbranch_scc1 .Lfd_p1e\n"
    "v_mov_b32 v64, 0\n"
    "v_mov_b32 v65, v61\n"
    "v_mov_b32 v66, 0\n"
    "v_mov_b32 v67, v63\n"
    "v_add_u32 v68, 11008, v8\n"
    "s_mov_b64 exec, s[46:47]\n"
    "global_store_dwordx4 v68, v[64:67], s[6:7] sc1\n"
    "s_mov_b64 exec, -1\n"
    ".Lfd_p1e:\n"
    "s_nop 7\n"
    "v_cvt_pk_bf16_f32 v60, v88, v89\n"
    "v_cvt_pk_bf16_f32 v62, v90, v91\n"
    "s_mov_b64 exec, s[46:47]\n"
    ".Lfd_p1s:\n"
    "global_store_dwordx4 v8, v[60:63], s[6:7] sc1\n"
    "s_mov_b64 exec, -1\n"
    "s_cmp_lt_u32 s29, 4\n"
    "s_cbranch_scc1 .Lfd_p2\n"
    "s_mov_b64 exec, 0\n"
    "s_cmp_eq_u32 s29, 5\n"
    "s_cbranch_scc1 .Lfd_zB0\n"
    "s_cmp_eq_u32 s29, 6\n"
    "s_cbranch_scc1 .Lfd_zC0\n"
    "s_cmp_eq_u32 s29, 4\n"
    "s_cbranch_scc1 .Lfd_zA0\n"
    ".Lfd_dryret:\n"
    "s_mov_b64 exec, -1\n"
    ".Lfd_p2:\n"
    "s_lshl_b32 s37, s29, 3\n"
    "s_add_u32 s37, s37, 40\n"
    "s_cmp_ge_u32 s41, s37\n"
    "s_cbranch_scc1 .Lfd_s17\n"
    ".Lfd_w18:\n"
    "ds_read_b32 v27, v5 offset:0\n"
    "s_waitcnt lgkmcnt(0)\n"
    "v_readfirstlane_b32 s41, v27\n"
    "s_cmp_ge_u32 s41, s37\n"
    "s_cbranch_scc1 .Lfd_d19\n"
    "s_sleep 1\n"
    "s_sub_i32 s40, s40, 1\n"
    "s_cmp_gt_i32 s40, 0\n"
    "s_cbranch_scc1 .Lfd_w18\n"
    ".Lfd_d19:\n"
    ".Lfd_s17:\n"
    "ds_read_b128 v[28:31], v9\n"
    "ds_read_b128 v[32:35], v9 offset:4096\n"
    "ds_read_b128 v[36:39], v10\n"
    "ds_read_b128 v[40:43], v10 offset:4096\n"
    "ds_read_b128 v[44:47], v11\n"
    "ds_read_b128 v[48:51], v11 offset:4096\n"
    "ds_read_b128 v[52:55], v12\n"
    "ds_read_b128 v[56:59], v12 offset:4096\n"
    ".Lfd_w20:\n"
    "ds_read_b32 v27, v5 offset:4\n"
    "s_waitcnt lgkmcnt(0)\n"
    "v_readfirstlane_b32 s38, v27\n"
    "s_cmp_ge_u32 s38, 1\n"
    "s_cbranch_scc1 .Lfd_d21\n"
    "s_sleep 1\n"
    "s_sub_i32 s40, s40, 1\n"
    "s_cmp_gt_i32 s40, 0\n"
    "s_cbranch_scc1 .Lfd_w20\n"
    ".Lfd_d21:\n"
    ".Lfd_zA0:\n"
    "s_cbranch_execz .Lfd_zA1\n"
    "ds_read_b128 v[72:75], v13 offset:1280\n"
    "ds_read_b128 v[76:79], v13 offset:1344\n"
    "ds_read_b128 v[80:83], v13 offset:1408\n"
    "ds_read_b128 v[84:87], v13 offset:1472\n"
    "s_waitcnt lgkmcnt(3)\n"
    "v_mfma_f32_16x16x32_bf16 v[88:91], v[28:31], v[72:75], 0\n"
    "v_mfma_f32_16x16x32_bf16 v[92:95], v[32:35], v[72:75], 0\n"
    "s_waitcnt lgkmcnt(2)\n"
    ".Lfd_zA1:\n"
    "s_cbranch_execz .Lfd_zA2\n"
    "v_mfma_f32_16x16x32_bf16 v[88:91], v[36:39], v[76:79], v[88:91]\n"
    "v_mfma_f32_16x16x32_bf16 v[92:95], v[40:43], v[76:79], v[92:95]\n"
    "s_waitcnt lgkmcnt(1)\n"
    "v_mfma_f32_16x16x32_bf16 v[88:91], v[44:47], v[80:83], v[88:91]\n"
    "v_mfma_f32_16x16x32_bf16 v[92:95], v[48:51], v[80:83], v[92:95]\n"
    "s_waitcnt lgkmcnt(0)\n"
    "v_mfma_f32_16x16x32_bf16 v[88:91], v[52:55], v[84:87], v[88:91]\n"
    "v_mfma_f32_16x16x32_bf16 v[92:95], v[56:59], v[84:87], v[92:95]\n"
    ".Lfd_zA2:\n"
    "s_cbranch_execz .Lfd_zA3\n"
    "s_mov_b32 s37, 0x3db504f3\n"
    "s_mov_b32 s39, 0x3fb8aa3b\n"
    "s_nop 5\n"
    "v_cndmask_b32 v28, v88, v92, s[52:53]\n"
    "v_cndmask_b32 v29, v89, v93, s[52:53]\n"
    "v_cndmask_b32 v30, v90, v94, s[52:53]\n"
    "v_cndmask_b32 v31, v91, v95, s[52:53]\n"
    "v_cvt_pk_bf16_f32 v28, v28, v29\n"
    ".Lfd_zA3:\n"
    "s_cbranch_execz .Lfd_zA4\n"
    "v_cvt_pk_bf16_f32 v30, v30, v31\n"
    "v_and_b32 v29, 0xffff0000, v28\n"
    "v_lshlrev_b32 v28, 16, v28\n"
    "v_and_b32 v31, 0xffff0000, v30\n"
    "v_lshlrev_b32 v30, 16, v30\n"
    "v_mul_f32 v28, s37, v28\n"
    "v_mul_f32 v29, s37, v29\n"
    "v_mul_f32 v30, s37, v30\n"
    "v_mul_f32 v31, s37, v31\n"
    "v_cvt_pk_bf16_f32 v28, v28, v29\n"
    ".Lfd_zA4:\n"
    "s_cbranch_execz .Lfd_zA5\n"
    "v_cvt_pk_bf16_f32 v29, v30, v31\n"
    "s_mov_b64 exec, s[46:47]\n"
    "ds_write_b64 v14, v[28:29]\n"
    "s_mov_b64 exec, 1\n"
    "ds_add_u32 v5, v26 offset:12\n"
    "s_mov_b64 exec, -1\n"
    ".Lfd_w22:\n"
    "ds_read_b32 v27, v5 offset:12\n"
    "ds_read_b64 v[72:73], v15 offset:1536\n"
    "s_waitcnt lgkmcnt(0)\n"
    "v_readfirstlane_b32 s38, v27\n"
    ".Lfd_zA5:\n"
    "s_cbranch_execz .Lfd_zA6\n"
    "s_cmp_ge_u32 s38, 8\n"
    "s_cbranch_scc1 .Lfd_d23\n"
    "s_sub_i32 s40, s40, 1\n"
    "s_cmp_gt_i32 s40, 0\n"
    "s_cbranch_scc1 .Lfd_w22\n"
    ".Lfd_d23:\n"
    "v_lshlrev_b32 v74, 16, v72\n"
    "v_and_b32 v75, 0xffff0000, v72\n"
    "v_lshlrev_b32 v76, 16, v73\n"
    "v_and_b32 v77, 0xffff0000, v73\n"
    "v_max3_f32 v78, v74, v75, v76\n"
    "v_max_f32 v78, v78, v77\n"
    "s_nop 1\n"
    ".Lfd_zA6:\n"
    "s_cbranch_execz .Lfd_zA7\n"
    "v_max_f32_dpp v78, v78, v78 quad_perm:[1,0,3,2] row_mask:0xf bank_mask:0xf\n"
    "s_nop 1\n"
    "v_max_f32_dpp v78, v78, v78 quad_perm:[2,3,0,1] row_mask:0xf bank_mask:0xf\n"
    "s_nop 1\n"
    "v_max_f32_dpp v78, v78, v78 row_half_mirror row_mask:0xf bank_mask:0xf\n"
    "s_nop 1\n"
    "v_max_f32_dpp v78, v78, v78 row_mirror row_mask:0xf bank_mask:0xf\n"
    "v_mov_b32 v79, v78\n"
    "s_nop 1\n"
    "v_permlane16_swap_b32 v78, v79\n"
    "s_nop 0\n"
    ".Lfd_zA7:\n"
    "s_cbranch_execz .Lfd_zA8\n"
    "v_max_f32 v78, v78, v79\n"
    "v_mov_b32 v79, v78\n"
    "s_nop 1\n"
    "v_permlane32_swap_b32 v78, v79\n"
    "s_nop 0\n"
    "v_max_f32 v60, v78, v79\n"
    "v_sub_f32 v74, v74, v60\n"
    "v_sub_f32 v75, v75, v60\n"
    "v_sub_f32 v76, v76, v60\n"
    "v_sub_f32 v77, v77, v60\n"
    "v_mul_f32 v74, s39, v74\n"
    "v_mul_f32 v75, s39, v75\n"
    "v_mul_f32 v76, s39, v76\n"
    "v_mul_f32 v77, s39, v77\n"
    "v_exp_f32 v74, v74\n"
    ".Lfd_zA8:\n"
    "s_cbranch_execz .Lfd_zA9\n"
    "v_exp_f32 v75, v75\n"
    "v_exp_f32 v76, v76\n"
    "v_exp_f32 v77, v77\n"
    "v_cvt_pk_bf16_f32 v72, v74, v75\n"
    "s_nop 0\n"
    "v_cvt_pk_bf16_f32 v73, v76, v77\n"
    "ds_write_b64 v15, v[72:73] offset:2048\n"
    "ds_read_b128 v[28:31], v13 offset:2048\n"
    "ds_read_b128 v[32:35], v13 offset:2112\n"
    ".Lfd_zA9:\n"
    "s_cbranch_execz .Lfd_zA10\n"
    "ds_read_b128 v[36:39], v13 offset:2176\n"
    "ds_read_b128 v[40:43], v13 offset:2240\n"
    "ds_read_b128 v[44:47], v13 offset:2304\n"
    "ds_read_b128 v[48:51], v13 offset:2368\n"
    "ds_read_b128 v[52:55], v13 offset:2432\n"
    "ds_read_b128 v[56:59], v13 offset:2496\n"
    "s_cmp_lg_u32 s29, 0\n"
    "s_cbranch_scc1 .Lfd_p3\n"
    "v_add_f32 v74, v74, v75\n"
    ".Lfd_zA10:\n"
    "s_cbranch_execz .Lfd_zA11\n"
    "v_add_f32 v76, v76, v77\n"
    "v_add_f32 v74, v74, v76\n"
    "s_nop 1\n"
    "v_add_f32_dpp v74, v74, v74 quad_perm:[1,0,3,2] row_mask:0xf bank_mask:0xf\n"
    "s_nop 1\n"
    "v_add_f32_dpp v74, v74, v74 quad_perm:[2,3,0,1] row_mask:0xf bank_mask:0xf\n"
    "s_nop 1\n"
    "v_add_f32_dpp v74, v74, v74 row_half_mirror row_mask:0xf bank_mask:0xf\n"
    "s_nop 1\n"
    "v_add_f32_dpp v74, v74, v74 row_mirror row_mask:0xf bank_mask:0xf\n"
    "v_mov_b32 v75, v74\n"
    ".Lfd_zA11:\n"
    "s_cbranch_execz .Lfd_zA12\n"
    "s_nop 1\n"
    "v_permlane16_swap_b32 v74, v75\n"
    "s_nop 0\n"
    "v_add_f32 v74, v74, v75\n"
    "v_mov_b32 v75, v74\n"
    "s_nop 1\n"
    "v_permlane32_swap_b32 v74, v75\n"
    "v_mov_b32 v76, s99\n"
    "v_add_f32 v62, v74, v75\n"
    "s_mov_b64 exec, 1\n"
    "global_store_dwordx4 v76, v[60:63], s[6:7] sc1\n"
    "s_mov_b64 exec, -1\n"
    ".Lfd_p3:\n"
    ".Lfd_zA12:\n"
    "s_cbranch_execz .Lfd_dryret\n"
    ".Lfd_zB0:\n"
    "s_cbranch_execz .Lfd_zB1\n"
    ".Lfd_w24:\n"
    "ds_read_b32 v27, v5 offset:0\n"
    "ds_read_b64_tr_b16 v[72:73], v16 offset:0\n"
    "ds_read_b64_tr_b16 v[74:75], v16 offset:1024\n"
    "s_waitcnt lgkmcnt(0)\n"
    "v_readfirstlane_b32 s41, v27\n"
    "s_cmp_ge_u32 s41, 104\n"
    "s_cbranch_scc1 .Lfd_d25\n"
    "s_sub_i32 s40, s40, 1\n"
    "s_cmp_gt_i32 s40, 0\n"
    "s_cbranch_scc1 .Lfd_w24\n"
    ".Lfd_d25:\n"
    ".Lfd_zB1:\n"
    "s_cbranch_execz .Lfd_zB2\n"
    "v_mfma_f32_16x16x32_bf16 v[88:91], v[72:75], v[28:31], 0\n"
    ".Lfd_w27:\n"
    "ds_read_b32 v27, v5 offset:0\n"
    "ds_read_b64_tr_b16 v[76:77], v16 offset:8192\n"
    "ds_read_b64_tr_b16 v[78:79], v16 offset:9216\n"
    "s_waitcnt lgkmcnt(0)\n"
    "v_readfirstlane_b32 s41, v27\n"
    "s_cmp_ge_u32 s41, 112\n"
    "s_cbranch_scc1 .Lfd_d28\n"
    "s_sub_i32 s40, s40, 1\n"
    "s_cmp_gt_i32 s40, 0\n"
    ".Lfd_zB2:\n"
    "s_cbranch_execz .Lfd_zB3\n"
    "s_cbranch_scc1 .Lfd_w27\n"
    ".Lfd_d28:\n"
    "v_mfma_f32_16x16x32_bf16 v[88:91], v[76:79], v[32:35], v[88:91]\n"
    ".Lfd_w30:\n"
    "ds_read_b32 v27, v5 offset:0\n"
    "ds_read_b64_tr_b16 v[72:73], v16 offset:16384\n"
    "ds_read_b64_tr_b16 v[74:75], v16 offset:17408\n"
    "s_waitcnt lgkmcnt(0)\n"
    "v_readfirstlane_b32 s41, v27\n"
    "s_cmp_ge_u32 s41, 120\n"
    "s_cbranch_scc1 .Lfd_d31\n"
    "s_sub_i32 s40, s40, 1\n"
    ".Lfd_zB3:\n"
    "s_cbranch_execz .Lfd_zB4\n"
    "s_cmp_gt_i32 s40, 0\n"
    "s_cbranch_scc1 .Lfd_w30\n"
    ".Lfd_d31:\n"
    "v_mfma_f32_16x16x32_bf16 v[88:91], v[72:75], v[36:39], v[88:91]\n"
    ".Lfd_w33:\n"
    "ds_read_b32 v27, v5 offset:0\n"
    "ds_read_b64_tr_b16 v[76:77], v16 offset:24576\n"
    "ds_read_b64_tr_b16 v[78:79], v16 offset:25600\n"
    "s_waitcnt lgkmcnt(0)\n"
    "v_readfirstlane_b32 s41, v27\n"
    "s_cmp_ge_u32 s41, 128\n"
    "s_cbranch_scc1 .Lfd_d34\n"
    ".Lfd_zB4:\n"
    "s_cbranch_execz .Lfd_zB5\n"
    "s_sub_i32 s40, s40, 1\n"
    "s_cmp_gt_i32 s40, 0\n"
    "s_cbranch_scc1 .Lfd_w33\n"
    ".Lfd_d34:\n"
    "v_mfma_f32_16x16x32_bf16 v[88:91], v[76:79], v[40:43], v[88:91]\n"
    ".Lfd_w36:\n"
    "ds_read_b32 v27, v5 offset:0\n"
    "ds_read_b64_tr_b16 v[72:73], v16 offset:32768\n"
    "ds_read_b64_tr_b16 v[74:75], v16 offset:33792\n"
    "s_waitcnt lgkmcnt(0)\n"
    "v_readfirstlane_b32 s41, v27\n"
    "s_cmp_ge_u32 s41, 136\n"
    ".Lfd_zB5:\n"
    "s_cbranch_execz .Lfd_zB6\n"
    "s_cbranch_scc1 .Lfd_d37\n"
    "s_sub_i32 s40, s40, 1\n"
    "s_cmp_gt_i32 s40, 0\n"
    "s_cbranch_scc1 .Lfd_w36\n"
    ".Lfd_d37:\n"
    "v_mfma_f32_16x16x32_bf16 v[88:91], v[72:75], v[44:47], v[88:91]\n"
    ".Lfd_w39:\n"
    "ds_read_b32 v27, v5 offset:0\n"
    "ds_read_b64_tr_b16 v[76:77], v16 offset:40960\n"
    "ds_read_b64_tr_b16 v[78:79], v16 offset:41984\n"
    "s_waitcnt lgkmcnt(0)\n"
    "v_readfirstlane_b32 s41, v27\n"
    ".Lfd_zB6:\n"
    "s_cbranch_execz .Lfd_zB7\n"
    "s_cmp_ge_u32 s41, 144\n"
    "s_cbranch_scc1 .Lfd_d40\n"
    "s_sub_i32 s40, s40, 1\n"
    "s_cmp_gt_i32 s40, 0\n"
    "s_cbranch_scc1 .Lfd_w39\n"
    ".Lfd_d40:\n"
    "v_mfma_f32_16x16x32_bf16 v[88:91], v[76:79], v[48:51], v[88:91]\n"
    ".Lfd_w42:\n"
    "ds_read_b32 v27, v5 offset:0\n"
    "ds_read_b64_tr_b16 v[72:73], v16 offset:49152\n"
    "ds_read_b64_tr_b16 v[74:75], v16 offset:50176\n"
    "s_waitcnt lgkmcnt(0)\n"
    ".Lfd_zB7:\n"
    "s_cbranch_execz .Lfd_zB8\n"
    "v_readfirstlane_b32 s41, v27\n"
    "s_cmp_ge_u32 s41, 152\n"
    "s_cbranch_scc1 .Lfd_d43\n"
    "s_sub_i32 s40, s40, 1\n"
    "s_cmp_gt_i32 s40, 0\n"
    "s_cbranch_scc1 .Lfd_w42\n"
    ".Lfd_d43:\n"
    "v_mfma_f32_16x16x32_bf16 v[88:91], v[72:75], v[52:55], v[88:91]\n"
    ".Lfd_w45:\n"
    "ds_read_b32 v27, v5 offset:0\n"
    "ds_read_b64_tr_b16 v[76:77], v16 offset:57344\n"
    "ds_read_b64_tr_b16 v[78:79], v16 offset:58368\n"
    ".Lfd_zB8:\n"
    "s_cbranch_execz .Lfd_zB9\n"
    "s_waitcnt lgkmcnt(0)\n"
    "v_readfirstlane_b32 s41, v27\n"
    "s_cmp_ge_u32 s41, 160\n"
    "s_cbranch_scc1 .Lfd_d46\n"
    "s_sub_i32 s40, s40, 1\n"
    "s_cmp_gt_i32 s40, 0\n"
    "s_cbranch_scc1 .Lfd_w45\n"
    ".Lfd_d46:\n"
    "v_mfma_f32_16x16x32_bf16 v[88:91], v[76:79], v[56:59], v[88:91]\n"
    ".Lfd_zB9:\n"
    "s_cbranch_execz .Lfd_dryret\n"
    ".Lfd_zC0:\n"
    "s_cbranch_execz .Lfd_zC1\n"
    "s_nop 5\n"
    "v_cndmask_b32 v60, v88, v90, s[52:53]\n"
    "v_cndmask_b32 v62, v89, v91, s[52:53]\n"
    "s_mov_b64 exec, s[46:47]\n"
    "global_store_dwordx4 v17, v[60:63], s[6:7] sc1\n"
    "s_mov_b64 exec, -1\n"
    "global_load_dwordx4 v[68:71], v19, s[6:7] sc1\n"
    "s_cmp_ge_u32 s41, 174\n"
    "s_cbranch_scc1 .Lfd_s48\n"
    ".Lfd_w49:\n"
    ".Lfd_zC1:\n"
    "s_cbranch_execz .Lfd_zC2\n"
    "ds_read_b32 v27, v5 offset:0\n"
    "s_waitcnt lgkmcnt(0)\n"
    "v_readfirstlane_b32 s41, v27\n"
    "s_cmp_ge_u32 s41, 174\n"
    "s_cbranch_scc1 .Lfd_d50\n"
    "s_sub_i32 s40, s40, 1\n"
    "s_cmp_gt_i32 s40, 0\n"
    "s_cbranch_scc1 .Lfd_w49\n"
    ".Lfd_d50:\n"
    ".Lfd_s48:\n"
    "global_load_dwordx4 v[64:67], v18, s[6:7] sc1\n"
    ".Lfd_p51:\n"
    "s_waitcnt vmcnt(1)\n"
    "v_cmp_ne_u32 vcc, s42, v69\n"
    ".Lfd_zC2:\n"
    "s_cbranch_execz .Lfd_zC3\n"
    "v_cmp_ne_u32 s[54:55], s43, v71\n"
    "s_or_b64 vcc, vcc, s[54:55]\n"
    "s_cbranch_vccz .Lfd_e52\n"
    "s_sub_i32 s40, s40, 16\n"
    "s_cmp_gt_i32 s40, 0\n"
    "s_cbranch_scc0 .Lfd_e52\n"
    "s_mov_b64 exec, vcc\n"
    "global_load_dwordx4 v[68:71], v19, s[6:7] offset:0 sc1\n"
    "s_waitcnt vmcnt(0)\n"
    "s_branch .Lfd_p51\n"
    ".Lfd_e52:\n"
    "s_mov_b64 exec, -1\n"
    "v_mov_b32 v44, v68\n"
    "s_nop 1\n"
    ".Lfd_zC3:\n"
    "s_cbranch_execz .Lfd_zC4\n"
    "v_max_f32_dpp v44, v44, v44 quad_perm:[1,0,3,2] row_mask:0xf bank_mask:0xf\n"
    "s_nop 1\n"
    "v_max_f32_dpp v44, v44, v44 quad_perm:[2,3,0,1] row_mask:0xf bank_mask:0xf\n"
    "v_sub_f32 v44, v68, v44\n"
    "v_mul_f32 v44, s39, v44\n"
    "v_exp_f32 v44, v44\n"
    "s_nop 0\n"
    "v_mul_f32 v45, v44, v70\n"
    "s_nop 1\n"
    "v_add_f32_dpp v45, v45, v45 quad_perm:[1,0,3,2] row_mask:0xf bank_mask:0xf\n"
    "s_nop 1\n"
    ".Lfd_zC4:\n"
    "s_cbranch_execz .Lfd_zC5\n"
    "v_add_f32_dpp v45, v45, v45 quad_perm:[2,3,0,1] row_mask:0xf bank_mask:0xf\n"
    "v_rcp_f32 v45, v45\n"
    "s_cmp_ge_u32 s29, 4\n"
    "v_mul_f32 v44, v44, v45\n"
    ".Lfd_p53:\n"
    "s_waitcnt vmcnt(0)\n"
    "v_cmp_ne_u32 vcc, s42, v65\n"
    "v_cmp_ne_u32 s[54:55], s43, v67\n"
    "s_or_b64 vcc, vcc, s[54:55]\n"
    "s_cbranch_vccz .Lfd_e54\n"
    "s_sub_i32 s40, s40, 16\n"
    "s_cmp_gt_i32 s40, 0\n"
    "s_cbranch_scc0 .Lfd_e54\n"
    "s_mov_b64 exec, vcc\n"
    ".Lfd_zC5:\n"
    "s_cbranch_execz .Lfd_zC6\n"
    "global_load_dwordx4 v[64:67], v18, s[6:7] offset:0 sc1\n"
    "s_waitcnt vmcnt(0)\n"
    "s_branch .Lfd_p53\n"
    ".Lfd_e54:\n"
    "s_mov_b64 exec, -1\n"
    "v_mul_f32 v64, v44, v64\n"
    "v_mul_f32 v66, v44, v66\n"
    "s_cmp_ge_u32 s29, 4\n"
    "v_add_f32_dpp v64, v64, v64 quad_perm:[1,0,3,2] row_mask:0xf bank_mask:0xf\n"
    "v_add_f32_dpp v66, v66, v66 quad_perm:[1,0,3,2] row_mask:0xf bank_mask:0xf\n"
    "s_nop 0\n"
    "v_add_f32_dpp v64, v64, v64 quad_perm:[2,3,0,1] row_mask:0xf bank_mask:0xf\n"
    ".Lfd_zC6:\n"
    "s_cbranch_execz .Lfd_zC7\n"
    "v_add_f32_dpp v66, v66, v66 quad_perm:[2,3,0,1] row_mask:0xf bank_mask:0xf\n"
    "v_cvt_pk_bf16_f32 v64, v64, v66\n"
    "ds_write_b32 v20, v64\n"
    "s_cbranch_scc0 .Lfd_p5\n"
    "s_waitcnt lgkmcnt(0)\n"
    "s_barrier\n"
    "s_branch .Lfd_cend\n"
    ".Lfd_p5:\n"
    "s_waitcnt lgkmcnt(0)\n"
    "s_barrier\n"
    "ds_read_b64_tr_b16 v[28:29], v7 offset:0\n"
    ".Lfd_zC7:\n"
    "s_cbranch_execz .Lfd_zC8\n"
    "ds_read_b64_tr_b16 v[30:31], v7 offset:512\n"
    "ds_read_b128 v[44:47], v21 offset:512\n"
    "ds_read_b64_tr_b16 v[32:33], v7 offset:1024\n"
    "ds_read_b64_tr_b16 v[34:35], v7 offset:1536\n"
    "ds_read_b128 v[48:51], v21 offset:576\n"
    "ds_read_b64_tr_b16 v[36:37], v7 offset:2048\n"
    "ds_read_b64_tr_b16 v[38:39], v7 offset:2560\n"
    ".Lfd_zC8:\n"
    "s_cbranch_execz .Lfd_zC9\n"
    "ds_read_b128 v[52:55], v21 offset:640\n"
    "ds_read_b64_tr_b16 v[40:41], v7 offset:3072\n"
    "ds_read_b64_tr_b16 v[42:43], v7 offset:3584\n"
    "ds_read_b128 v[56:59], v21 offset:704\n"
    "ds_read_b128 v[72:75], v22 offset:1024\n"
    "ds_read_b64 v[76:77], v23\n"
    "s_waitcnt lgkmcnt(11)\n"
    "v_mfma_f32_16x16x32_bf16 v[92:95], v[28:31], v[44:47], 0\n"
    ".Lfd_zC9:\n"
    "s_cbranch_execz .Lfd_zC10\n"
    "ds_read_b64_tr_b16 v[80:81], v24 offset:16384\n"
    "ds_read_b64_tr_b16 v[82:83], v24 offset:16896\n"
    "ds_read_b64_tr_b16 v[78:79], v25\n"
    "s_waitcnt lgkmcnt(11)\n"
    "v_mfma_f32_16x16x32_bf16 v[92:95], v[32:35], v[48:51], v[92:95]\n"
    "s_waitcnt lgkmcnt(8)\n"
    "v_mfma_f32_16x16x32_bf16 v[92:95], v[36:39], v[52:55], v[92:95]\n"
    "s_waitcnt lgkmcnt(5)\n"
    "v_mfma_f32_16x16x32_bf16 v[92:95], v[40:43], v[56:59], v[92:95]\n"
    ".Lfd_zC10:\n"
    "s_cbranch_execz .Lfd_zC11\n"
    "s_waitcnt lgkmcnt(0)\n"
    "v_mfma_f32_16x16x16_bf16 v[88:91], v[78:79], v[76:77], 0\n"
    "v_mfma_f32_16x16x32_bf16 v[92:95], v[80:83], v[72:75], v[92:95]\n"
    "s_nop 6\n"
    "v_cndmask_b32 v88, v88, v90, s[50:51]\n"
    "v_cndmask_b32 v89, v89, v91, s[50:51]\n"
    "v_cndmask_b32 v60, v92, v94, s[50:51]\n"
    "v_cndmask_b32 v62, v93, v95, s[50:51]\n"
    "v_add_f32 v60, v60, v88\n"
    ".Lfd_zC11:\n"
    "s_cbranch_execz .Lfd_zC12\n"
    "v_add_f32 v62, v62, v89\n"
    "s_mov_b64 exec, s[44:45]\n"
    "global_store_dwordx4 v6, v[60:63], s[6:7] sc1\n"
    "s_mov_b64 exec, -1\n"
    ".Lfd_zC12:\n"
    "s_cbranch_execz .Lfd_dryret\n"
    ".Lfd_cend:\n"
    "s_endpgm\n"
    ".Lfd_aux:\n"
    "s_lshr_b32 s34, s24, 3\n"
    "s_bfe_u32 s35, s24, 0x10002\n"
    "s_lshl_b32 s36, s35, 10\n"
    "s_lshl_b32 s37, s25, 7\n"
    "s_add_u32 s36, s36, s37\n"
    "s_lshl_b32 s37, s34, 5\n"
    "s_add_u32 s36, s36, s37\n"
    "v_and_b32 v6, 7, v0\n"
    "v_lshl_add_u32 v6, v6, 2, s36\n"
    "v_add_u32 v7, 126976, v6\n"
    "s_lshl2_add_u32 s37, s25, s34\n"
    "s_lshl1_add_u32 s37, s37, s35\n"
    "s_lshl_b32 s37, s37, 12\n"
    "s_add_u32 s37, s37, 851968\n"
    "v_lshl_add_u32 v8, v0, 4, s37\n"
    "s_lshl_b32 s37, s24, 9\n"
    "s_add_u32 s37, s37, 16384\n"
    "v_lshl_add_u32 v9, v0, 4, s37\n"
    "v_lshlrev_b32 v10, 3, v0\n"
    "v_add_u32 v11, 160800, v10\n"
    "v_add_u32 v10, 160768, v10\n"
    "v_cmp_gt_u32 vcc, 12, v0\n"
    "s_mov_b32 s44, 0xffffff\n"
    "s_mov_b32 s45, 0\n"
    "v_cndmask_b32 v10, v11, v10, vcc\n"
    "v_lshrrev_b32 v14, 5, v0\n"
    "v_and_b32 v13, 7, v0\n"
    "v_lshlrev_b32 v13, 5, v13\n"
    "v_lshl_add_u32 v13, v14, 8, v13\n"
    "v_add_u32 v13, 159744, v13\n"
    "v_lshlrev_b32 v14, 4, v0\n"
    "v_add_u32 v14, 122880, v14\n"
    "s_waitcnt lgkmcnt(0)\n"
    ".Lfd_w55:\n"
    "ds_read_b32 v27, v5 offset:24\n"
    "s_waitcnt lgkmcnt(0)\n"
    "v_readfirstlane_b32 s38, v27\n"
    "s_cmp_ge_u32 s38, 1\n"
    "s_cbranch_scc1 .Lfd_d56\n"
    "s_sub_i32 s40, s40, 1\n"
    "s_cmp_gt_i32 s40, 0\n"
    "s_cbranch_scc1 .Lfd_w55\n"
    ".Lfd_d56:\n"
    "v_lshlrev_b32 v34, 5, v0\n"
    "v_add_u32 v34, 126976, v34\n"
    "ds_read_b128 v[28:31], v34\n"
    "ds_read_b128 v[32:35], v34 offset:16\n"
    "ds_read_b64 v[62:63], v5 offset:32\n"
    "ds_read_b32 v7, v7\n"
    "v_mov_b32 v40, 0\n"
    "v_mov_b32 v41, 0\n"
    "s_waitcnt lgkmcnt(2)\n"
    "v_dot2c_f32_bf16 v40, 0x3f803f80, v28\n"
    "v_dot2c_f32_bf16 v41, 0x3f803f80, v32\n"
    "v_dot2c_f32_bf16 v40, 0x3f803f80, v29\n"
    "v_dot2c_f32_bf16 v41, 0x3f803f80, v33\n"
    "v_dot2c_f32_bf16 v40, 0x3f803f80, v30\n"
    "v_dot2c_f32_bf16 v41, 0x3f803f80, v34\n"
    "v_dot2c_f32_bf16 v40, 0x3f803f80, v31\n"
    "v_dot2c_f32_bf16 v41, 0x3f803f80, v35\n"
    "v_lshlrev_b32 v44, 16, v28\n"
    "v_and_b32 v45, 0xffff0000, v28\n"
    "v_lshlrev_b32 v46, 16, v29\n"
    "v_and_b32 v47, 0xffff0000, v29\n"
    "v_lshlrev_b32 v48, 16, v30\n"
    "v_and_b32 v49, 0xffff0000, v30\n"
    "v_lshlrev_b32 v50, 16, v31\n"
    "v_and_b32 v51, 0xffff0000, v31\n"
    "v_lshlrev_b32 v52, 16, v32\n"
    "v_and_b32 v53, 0xffff0000, v32\n"
    "v_lshlrev_b32 v54, 16, v33\n"
    "v_and_b32 v55, 0xffff0000, v33\n"
    "v_lshlrev_b32 v56, 16, v34\n"
    "v_and_b32 v57, 0xffff0000, v34\n"
    "v_lshlrev_b32 v58, 16, v35\n"
    "v_and_b32 v59, 0xffff0000, v35\n"
    "v_add_f32 v36, v40, v41\n"
    "s_nop 1\n"
    "v_add_f32_dpp v36, v36, v36 quad_perm:[1,0,3,2] row_mask:0xf bank_mask:0xf\n"
    "s_nop 1\n"
    "v_add_f32_dpp v36, v36, v36 quad_perm:[2,3,0,1] row_mask:0xf bank_mask:0xf\n"
    "s_nop 1\n"
    "v_add_f32_dpp v36, v36, v36 row_half_mirror row_mask:0xf bank_mask:0xf\n"
    "s_nop 1\n"
    "v_add_f32_dpp v36, v36, v36 row_mirror row_mask:0xf bank_mask:0xf\n"
    "v_mov_b32 v37, v36\n"
    "s_nop 1\n"
    "v_permlane16_swap_b32 v36, v37\n"
    "s_nop 0\n"
    "v_add_f32 v36, v36, v37\n"
    "v_mul_f32 v36, 0x3b000000, v36\n"
    "v_cvt_pk_bf16_f32 v40, v36, v36\n"
    "v_and_b32 v40, 0xffff0000, v40\n"
    "v_sub_f32 v36, v36, v40\n"
    "v_pk_add_f32 v[44:45], v[44:45], v[40:41] op_sel_hi:[1,0] neg_lo:[0,1] neg_hi:[0,1]\n"
    "v_pk_add_f32 v[46:47], v[46:47], v[40:41] op_sel_hi:[1,0] neg_lo:[0,1] neg_hi:[0,1]\n"
    "v_pk_add_f32 v[48:49], v[48:49], v[40:41] op_sel_hi:[1,0] neg_lo:[0,1] neg_hi:[0,1]\n"
    "v_pk_add_f32 v[50:51], v[50:51], v[40:41] op_sel_hi:[1,0] neg_lo:[0,1] neg_hi:[0,1]\n"
    "v_pk_add_f32 v[52:53], v[52:53], v[40:41] op_sel_hi:[1,0] neg_lo:[0,1] neg_hi:[0,1]\n"
    "v_pk_add_f32 v[54:55], v[54:55], v[40:41] op_sel_hi:[1,0] neg_lo:[0,1] neg_hi:[0,1]\n"
    "v_pk_add_f32 v[56:57], v[56:57], v[40:41] op_sel_hi:[1,0] neg_lo:[0,1] neg_hi:[0,1]\n"
    "v_pk_add_f32 v[58:59], v[58:59], v[40:41] op_sel_hi:[1,0] neg_lo:[0,1] neg_hi:[0,1]\n"
    "v_pk_mul_f32 v[38:39], v[44:45], v[44:45]\n"
    "v_pk_mul_f32 v[42:43], v[46:47], v[46:47]\n"
    "v_pk_fma_f32 v[38:39], v[48:49], v[48:49], v[38:39]\n"
    "v_pk_fma_f32 v[42:43], v[50:51], v[50:51], v[42:43]\n"
    "v_pk_fma_f32 v[38:39], v[52:53], v[52:53], v[38:39]\n"
    "v_pk_fma_f32 v[42:43], v[54:55], v[54:55], v[42:43]\n"
    "v_pk_fma_f32 v[38:39], v[56:57], v[56:57], v[38:39]\n"
    "v_pk_fma_f32 v[42:43], v[58:59], v[58:59], v[42:43]\n"
    "v_pk_add_f32 v[38:39], v[38:39], v[42:43]\n"
    "v_add_f32 v42, v38, v39\n"
    "s_nop 1\n"
    "v_add_f32_dpp v42, v42, v42 quad_perm:[1,0,3,2] row_mask:0xf bank_mask:0xf\n"
    "s_nop 1\n"
    "v_add_f32_dpp v42, v42, v42 quad_perm:[2,3,0,1] row_mask:0xf bank_mask:0xf\n"
    "s_nop 1\n"
    "v_add_f32_dpp v42, v42, v42 row_half_mirror row_mask:0xf bank_mask:0xf\n"
    "s_nop 1\n"
    "v_add_f32_dpp v42, v42, v42 row_mirror row_mask:0xf bank_mask:0xf\n"
    "v_mov_b32 v43, v42\n"
    "s_nop 1\n"
    "v_permlane16_swap_b32 v42, v43\n"
    "s_nop 0\n"
    "v_add_f32 v42, v42, v43\n"
    "v_mul_f32 v42, 0x3b000000, v42\n"
    "v_fma_f32 v42, -v36, v36, v42\n"
    "v_max_f32 v42, 0, v42\n"
    "v_cvt_pk_bf16_f32 v42, v42, v42\n"
    "v_lshlrev_b32 v43, 16, v42\n"
    "v_add_f32 v43, 0x3727c5ac, v43\n"
    "v_cvt_pk_bf16_f32 v43, v43, v43\n"
    "v_lshlrev_b32 v43, 16, v43\n"
    "v_sqrt_f32 v43, v43\n"
    "s_nop 0\n"
    "v_cvt_pk_bf16_f32 v43, v43, v43\n"
    "v_lshlrev_b32 v43, 16, v43\n"
    "v_rcp_f32 v42, v43\n"
    "v_cvt_pk_bf16_f32 v28, v44, v45\n"
    "v_cvt_pk_bf16_f32 v29, v46, v47\n"
    "v_cvt_pk_bf16_f32 v30, v48, v49\n"
    "v_cvt_pk_bf16_f32 v31, v50, v51\n"
    "v_cvt_pk_bf16_f32 v32, v52, v53\n"
    "v_cvt_pk_bf16_f32 v33, v54, v55\n"
    "v_cvt_pk_bf16_f32 v34, v56, v57\n"
    "v_cvt_pk_bf16_f32 v35, v58, v59\n"
    "v_lshlrev_b32 v44, 16, v28\n"
    "v_and_b32 v45, 0xffff0000, v28\n"
    "v_lshlrev_b32 v46, 16, v29\n"
    "v_and_b32 v47, 0xffff0000, v29\n"
    "v_lshlrev_b32 v48, 16, v30\n"
    "v_and_b32 v49, 0xffff0000, v30\n"
    "v_lshlrev_b32 v50, 16, v31\n"
    "v_and_b32 v51, 0xffff0000, v31\n"
    "v_lshlrev_b32 v52, 16, v32\n"
    "v_and_b32 v53, 0xffff0000, v32\n"
    "v_lshlrev_b32 v54, 16, v33\n"
    "v_and_b32 v55, 0xffff0000, v33\n"
    "v_lshlrev_b32 v56, 16, v34\n"
    "v_and_b32 v57, 0xffff0000, v34\n"
    "v_lshlrev_b32 v58, 16, v35\n"
    "v_and_b32 v59, 0xffff0000, v35\n"
    "v_pk_mul_f32 v[44:45], v[44:45], v[42:43] op_sel_hi:[1,0]\n"
    "v_pk_mul_f32 v[46:47], v[46:47], v[42:43] op_sel_hi:[1,0]\n"
    "v_pk_mul_f32 v[48:49], v[48:49], v[42:43] op_sel_hi:[1,0]\n"
    "v_pk_mul_f32 v[50:51], v[50:51], v[42:43] op_sel_hi:[1,0]\n"
    "v_pk_mul_f32 v[52:53], v[52:53], v[42:43] op_sel_hi:[1,0]\n"
    "v_pk_mul_f32 v[54:55], v[54:55], v[42:43] op_sel_hi:[1,0]\n"
    "v_pk_mul_f32 v[56:57], v[56:57], v[42:43] op_sel_hi:[1,0]\n"
    "v_pk_mul_f32 v[58:59], v[58:59], v[42:43] op_sel_hi:[1,0]\n"
    "v_cvt_pk_bf16_f32 v28, v44, v45\n"
    "v_cvt_pk_bf16_f32 v29, v46, v47\n"
    "v_cvt_pk_bf16_f32 v30, v48, v49\n"
    "v_cvt_pk_bf16_f32 v31, v50, v51\n"
    "v_cvt_pk_bf16_f32 v32, v52, v53\n"
    "v_cvt_pk_bf16_f32 v33, v54, v55\n"
    "v_cvt_pk_bf16_f32 v34, v56, v57\n"
    "v_cvt_pk_bf16_f32 v35, v58, v59\n"
    "s_lshl_b32 s36, s27, 3\n"
    "s_lshl_b32 s36, 0xff, s36\n"
    "s_mov_b32 s37, s36\n"
    "s_mov_b64 exec, s[36:37]\n"
    "ds_write_b128 v13, v[28:31]\n"
    "ds_write_b128 v13, v[32:35] offset:16\n"
    "s_mov_b64 exec, -1\n"
    "s_mov_b32 s36, 0x05040100\n"
    "s_mov_b32 s37, 0x07060302\n"
    "v_perm_b32 v44, v29, v28, s36\n"
    "v_perm_b32 v48, v29, v28, s37\n"
    "v_perm_b32 v45, v31, v30, s36\n"
    "v_perm_b32 v49, v31, v30, s37\n"
    "v_perm_b32 v46, v33, v32, s36\n"
    "v_perm_b32 v50, v33, v32, s37\n"
    "v_perm_b32 v47, v35, v34, s36\n"
    "v_perm_b32 v51, v35, v34, s37\n"
    "ds_write_b128 v14, v[44:47]\n"
    "ds_write_b128 v14, v[48:51] offset:1024\n"
    "s_mov_b64 exec, 1\n"
    "ds_add_u32 v5, v26 offset:16\n"
    "s_mov_b64 exec, -1\n"
    "s_mov_b64 exec, 1\n"
    "ds_add_u32 v5, v26 offset:8\n"
    "s_mov_b64 exec, -1\n"
    "s_waitcnt lgkmcnt(0)\n"
    "v_readfirstlane_b32 s42, v62\n"
    "v_readfirstlane_b32 s43, v63\n"
    "s_add_u32 s42, s42, 0x7f4a7c15\n"
    "s_addc_u32 s43, s43, 0x9e3779b9\n"
    "v_mov_b32 v61, s42\n"
    "v_mov_b32 v63, s43\n"
    "s_and_b32 s36, s24, 3\n"
    "s_cbranch_scc1 .Lfd_auxret\n"
    "s_mov_b64 exec, 0\n"
    "s_branch .Lfd_zD0\n"
    ".Lfd_auxret:\n"
    "s_mov_b64 exec, -1\n"
    "s_lshl1_add_u32 s37, s24, s26\n"
    "s_lshl_b32 s37, s37, 12\n"
    "s_add_u32 s37, s37, 65536\n"
    "v_lshl_add_u32 v11, v0, 4, s37\n"
    "v_lshlrev_b32 v12, 2, v0\n"
    "v_add_u32 v12, 161024, v12\n"
    "s_setprio 3\n"
    ".Lfd_w57:\n"
    "ds_read_b32 v27, v5 offset:0\n"
    "s_waitcnt lgkmcnt(0)\n"
    "v_readfirstlane_b32 s38, v27\n"
    "s_cmp_ge_u32 s38, 68\n"
    "s_cbranch_scc1 .Lfd_d58\n"
    "s_sleep 1\n"
    "s_sub_i32 s40, s40, 1\n"
    "s_cmp_gt_i32 s40, 0\n"
    "s_cbranch_scc1 .Lfd_w57\n"
    ".Lfd_d58:\n"
    "global_load_dwordx4 v[28:31], v11, s[6:7] offset:0 sc1\n"
    "global_load_dwordx4 v[32:35], v11, s[6:7] offset:1024 sc1\n"
    "global_load_dwordx4 v[36:39], v11, s[6:7] offset:2048 sc1\n"
    "global_load_dwordx4 v[40:43], v11, s[6:7] offset:3072 sc1\n"
    ".Lfd_p59:\n"
    "s_waitcnt vmcnt(0)\n"
    "v_cmp_ne_u32 s[54:55], s42, v29\n"
    "v_cmp_ne_u32 vcc, s43, v31\n"
    "s_or_b64 s[54:55], s[54:55], vcc\n"
    "v_cmp_ne_u32 s[56:57], s42, v33\n"
    "v_cmp_ne_u32 vcc, s43, v35\n"
    "s_or_b64 s[56:57], s[56:57], vcc\n"
    "v_cmp_ne_u32 s[58:59], s42, v37\n"
    "v_cmp_ne_u32 vcc, s43, v39\n"
    "s_or_b64 s[58:59], s[58:59], vcc\n"
    "v_cmp_ne_u32 s[60:61], s42, v41\n"
    "v_cmp_ne_u32 vcc, s43, v43\n"
    "s_or_b64 s[60:61], s[60:61], vcc\n"
    "s_or_b64 vcc, s[54:55], s[56:57]\n"
    "s_or_b64 s[62:63], s[58:59], s[60:61]\n"
    "s_or_b64 vcc, vcc, s[62:63]\n"
    "s_cbranch_vccz .Lfd_e60\n"
    "s_sub_i32 s40, s40, 16\n"
    "s_cmp_gt_i32 s40, 0\n"
    "s_cbranch_scc0 .Lfd_e60\n"
    "s_mov_b64 exec, s[54:55]\n"
    "global_load_dwordx4 v[28:31], v11, s[6:7] offset:0 sc1\n"
    "s_mov_b64 exec, s[56:57]\n"
    "global_load_dwordx4 v[32:35], v11, s[6:7] offset:1024 sc1\n"
    "s_mov_b64 exec, s[58:59]\n"
    "global_load_dwordx4 v[36:39], v11, s[6:7] offset:2048 sc1\n"
    "s_mov_b64 exec, s[60:61]\n"
    "global_load_dwordx4 v[40:43], v11, s[6:7] offset:3072 sc1\n"
    "s_mov_b64 exec, -1\n"
    "s_branch .Lfd_p59\n"
    ".Lfd_e60:\n"
    "v_add_f32 v28, v28, v32\n"
    "v_add_f32 v30, v30, v34\n"
    "v_add_f32 v36, v36, v40\n"
    "v_add_f32 v38, v38, v42\n"
    "v_add_f32 v28, v28, v36\n"
    "v_add_f32 v30, v30, v38\n"
    "v_cvt_pk_bf16_f32 v28, v28, v30\n"
    "ds_write_b32 v12, v28\n"
    "s_mov_b64 exec, 1\n"
    "ds_add_u32 v5, v26 offset:4\n"
    "s_mov_b64 exec, -1\n"
    "s_setprio 0\n"
    "s_cmp_lg_u32 s25, 1\n"
    "s_cbranch_scc1 .Lfd_hx\n"
    "s_lshl_b32 s37, s28, 5\n"
    "s_add_u32 s37, s37, 327680\n"
    "v_lshl_add_u32 v11, v0, 4, s37\n"
    "v_add_u32 v12, 5312, v11\n"
    "v_cmp_gt_u32 vcc, 12, v0\n"
    "s_mov_b64 s[98:99], s[6:7]\n"
    "s_nop 0\n"
    "v_cndmask_b32 v11, v12, v11, vcc\n"
    ".Lfd_w61:\n"
    "ds_read_b32 v27, v5 offset:0\n"
    "s_waitcnt lgkmcnt(0)\n"
    "v_readfirstlane_b32 s38, v27\n"
    "s_cmp_ge_u32 s38, 80\n"
    "s_cbranch_scc1 .Lfd_d62\n"
    "s_sleep 1\n"
    "s_sub_i32 s40, s40, 1\n"
    "s_cmp_gt_i32 s40, 0\n"
    "s_cbranch_scc1 .Lfd_w61\n"
    ".Lfd_d62:\n"
    "s_mov_b64 exec, s[44:45]\n"
    ".Lfd_g63:\n"
    "s_mov_b64 s[98:99], s[6:7]\n"
    "global_load_dwordx4 v[28:31], v11, s[98:99] sc1\n"
    "s_add_u32 s98, s98, 11008\n"
    "s_addc_u32 s99, s99, 0\n"
    "global_load_dwordx4 v[32:35], v11, s[98:99] sc1\n"
    "s_add_u32 s98, s98, 11008\n"
    "s_addc_u32 s99, s99, 0\n"
    "global_load_dwordx4 v[36:39], v11, s[98:99] sc1\n"
    "s_add_u32 s98, s98, 11008\n"
    "s_addc_u32 s99, s99, 0\n"
    "global_load_dwordx4 v[40:43], v11, s[98:99] sc1\n"
    "s_add_u32 s98, s98, 11008\n"
    "s_addc_u32 s99, s99, 0\n"
    "global_load_dwordx4 v[44:47], v11, s[98:99] sc1\n"
    "s_add_u32 s98, s98, 11008\n"
    "s_addc_u32 s99, s99, 0\n"
    "global_load_dwordx4 v[48:51], v11, s[98:99] sc1\n"
    "s_add_u32 s98, s98, 11008\n"
    "s_addc_u32 s99, s99, 0\n"
    "global_load_dwordx4 v[52:55], v11, s[98:99] sc1\n"
    "s_add_u32 s98, s98, 11008\n"
    "s_addc_u32 s99, s99, 0\n"
    "global_load_dwordx4 v[56:59], v11, s[98:99] sc1\n"
    "s_add_u32 s98, s98, 11008\n"
    "s_addc_u32 s99, s99, 0\n"
    "global_load_dwordx4 v[64:67], v11, s[98:99] sc1\n"
    "s_add_u32 s98, s98, 11008\n"
    "s_addc_u32 s99, s99, 0\n"
    "global_load_dwordx4 v[68:71], v11, s[98:99] sc1\n"
    "s_add_u32 s98, s98, 11008\n"
    "s_addc_u32 s99, s99, 0\n"
    "global_load_dwordx4 v[72:75], v11, s[98:99] sc1\n"
    "s_add_u32 s98, s98, 11008\n"
    "s_addc_u32 s99, s99, 0\n"
    "global_load_dwordx4 v[76:79], v11, s[98:99] sc1\n"
    "s_waitcnt vmcnt(0)\n"
    "v_mov_b32 v16, 0\n"
    "v_xor_b32 v17, s42, v29\n"
    "v_xor_b32 v18, s43, v31\n"
    "v_or3_b32 v16, v16, v17, v18\n"
    "v_xor_b32 v17, s42, v33\n"
    "v_xor_b32 v18, s43, v35\n"
    "v_or3_b32 v16, v16, v17, v18\n"
    "v_xor_b32 v17, s42, v37\n"
    "v_xor_b32 v18, s43, v39\n"
    "v_or3_b32 v16, v16, v17, v18\n"
    "v_xor_b32 v17, s42, v41\n"
    "v_xor_b32 v18, s43, v43\n"
    "v_or3_b32 v16, v16, v17, v18\n"
    "v_xor_b32 v17, s42, v45\n"
    "v_xor_b32 v18, s43, v47\n"
    "v_or3_b32 v16, v16, v17, v18\n"
    "v_xor_b32 v17, s42, v49\n"
    "v_xor_b32 v18, s43, v51\n"
    "v_or3_b32 v16, v16, v17, v18\n"
    "v_xor_b32 v17, s42, v53\n"
    "v_xor_b32 v18, s43, v55\n"
    "v_or3_b32 v16, v16, v17, v18\n"
    "v_xor_b32 v17, s42, v57\n"
    "v_xor_b32 v18, s43, v59\n"
    "v_or3_b32 v16, v16, v17, v18\n"
    "v_xor_b32 v17, s42, v65\n"
    "v_xor_b32 v18, s43, v67\n"
    "v_or3_b32 v16, v16, v17, v18\n"
    "v_xor_b32 v17, s42, v69\n"
    "v_xor_b32 v18, s43, v71\n"
    "v_or3_b32 v16, v16, v17, v18\n"
    "v_xor_b32 v17, s42, v73\n"
    "v_xor_b32 v18, s43, v75\n"
    "v_or3_b32 v16, v16, v17, v18\n"
    "v_xor_b32 v17, s42, v77\n"
    "v_xor_b32 v18, s43, v79\n"
    "v_or3_b32 v16, v16, v17, v18\n"
    "v_cmp_ne_u32 vcc, 0, v16\n"
    "s_and_b64 vcc, vcc, exec\n"
    "s_cbranch_vccz .Lfd_h64\n"
    "s_sub_i32 s40, s40, 16\n"
    "s_cmp_gt_i32 s40, 0\n"
    "s_cbranch_scc1 .Lfd_g63\n"
    ".Lfd_h64:\n"
    "v_lshlrev_b32 v16, 16, v28\n"
    "v_and_b32 v80, 0xffff0000, v28\n"
    "v_lshlrev_b32 v17, 16, v32\n"
    "v_and_b32 v81, 0xffff0000, v32\n"
    "v_lshlrev_b32 v18, 16, v36\n"
    "v_and_b32 v82, 0xffff0000, v36\n"
    "v_lshlrev_b32 v19, 16, v40\n"
    "v_and_b32 v83, 0xffff0000, v40\n"
    "v_lshlrev_b32 v20, 16, v44\n"
    "v_and_b32 v84, 0xffff0000, v44\n"
    "v_lshlrev_b32 v21, 16, v48\n"
    "v_and_b32 v85, 0xffff0000, v48\n"
    "v_add_f32 v16, v16, v17\n"
    "v_add_f32 v18, v18, v19\n"
    "v_add_f32 v20, v20, v21\n"
    "v_add_f32 v80, v80, v81\n"
    "v_add_f32 v82, v82, v83\n"
    "v_add_f32 v84, v84, v85\n"
    "v_add_f32 v16, v16, v18\n"
    "v_add_f32 v80, v80, v82\n"
    "v_add_f32 v16, v16, v20\n"
    "v_add_f32 v80, v80, v84\n"
    "v_cvt_pk_bf16_f32 v16, v16, v80\n"
    "v_and_b32 v89, 0xffff0000, v16\n"
    "v_lshlrev_b32 v88, 16, v16\n"
    "v_lshlrev_b32 v16, 16, v30\n"
    "v_and_b32 v80, 0xffff0000, v30\n"
    "v_lshlrev_b32 v17, 16, v34\n"
    "v_and_b32 v81, 0xffff0000, v34\n"
    "v_lshlrev_b32 v18, 16, v38\n"
    "v_and_b32 v82, 0xffff0000, v38\n"
    "v_lshlrev_b32 v19, 16, v42\n"
    "v_and_b32 v83, 0xffff0000, v42\n"
    "v_lshlrev_b32 v20, 16, v46\n"
    "v_and_b32 v84, 0xffff0000, v46\n"
    "v_lshlrev_b32 v21, 16, v50\n"
    "v_and_b32 v85, 0xffff0000, v50\n"
    "v_add_f32 v16, v16, v17\n"
    "v_add_f32 v18, v18, v19\n"
    "v_add_f32 v20, v20, v21\n"
    "v_add_f32 v80, v80, v81\n"
    "v_add_f32 v82, v82, v83\n"
    "v_add_f32 v84, v84, v85\n"
    "v_add_f32 v16, v16, v18\n"
    "v_add_f32 v80, v80, v82\n"
    "v_add_f32 v16, v16, v20\n"
    "v_add_f32 v80, v80, v84\n"
    "v_cvt_pk_bf16_f32 v16, v16, v80\n"
    "v_and_b32 v91, 0xffff0000, v16\n"
    "v_lshlrev_b32 v90, 16, v16\n"
    "v_lshlrev_b32 v16, 16, v52\n"
    "v_and_b32 v80, 0xffff0000, v52\n"
    "v_lshlrev_b32 v17, 16, v56\n"
    "v_and_b32 v81, 0xffff0000, v56\n"
    "v_lshlrev_b32 v18, 16, v64\n"
    "v_and_b32 v82, 0xffff0000, v64\n"
    "v_lshlrev_b32 v19, 16, v68\n"
    "v_and_b32 v83, 0xffff0000, v68\n"
    "v_lshlrev_b32 v20, 16, v72\n"
    "v_and_b32 v84, 0xffff0000, v72\n"
    "v_lshlrev_b32 v21, 16, v76\n"
    "v_and_b32 v85, 0xffff0000, v76\n"
    "v_add_f32 v16, v16, v17\n"
    "v_add_f32 v18, v18, v19\n"
    "v_add_f32 v20, v20, v21\n"
    "v_add_f32 v80, v80, v81\n"
    "v_add_f32 v82, v82, v83\n"
    "v_add_f32 v84, v84, v85\n"
    "v_add_f32 v16, v16, v18\n"
    "v_add_f32 v80, v80, v82\n"
    "v_add_f32 v16, v16, v20\n"
    "v_add_f32 v80, v80, v84\n"
    "v_cvt_pk_bf16_f32 v16, v16, v80\n"
    "v_and_b32 v93, 0xffff0000, v16\n"
    "v_lshlrev_b32 v92, 16, v16\n"
    "v_lshlrev_b32 v16, 16, v54\n"
    "v_and_b32 v80, 0xffff0000, v54\n"
    "v_lshlrev_b32 v17, 16, v58\n"
    "v_and_b32 v81, 0xffff0000, v58\n"
    "v_lshlrev_b32 v18, 16, v66\n"
    "v_and_b32 v82, 0xffff0000, v66\n"
    "v_lshlrev_b32 v19, 16, v70\n"
    "v_and_b32 v83, 0xffff0000, v70\n"
    "v_lshlrev_b32 v20, 16, v74\n"
    "v_and_b32 v84, 0xffff0000, v74\n"
    "v_lshlrev_b32 v21, 16, v78\n"
    "v_and_b32 v85, 0xffff0000, v78\n"
    "v_add_f32 v16, v16, v17\n"
    "v_add_f32 v18, v18, v19\n"
    "v_add_f32 v20, v20, v21\n"
    "v_add_f32 v80, v80, v81\n"
    "v_add_f32 v82, v82, v83\n"
    "v_add_f32 v84, v84, v85\n"
    "v_add_f32 v16, v16, v18\n"
    "v_add_f32 v80, v80, v82\n"
    "v_add_f32 v16, v16, v20\n"
    "v_add_f32 v80, v80, v84\n"
    "v_cvt_pk_bf16_f32 v16, v16, v80\n"
    "v_and_b32 v95, 0xffff0000, v16\n"
    "v_lshlrev_b32 v94, 16, v16\n"
    "s_mov_b32 s36, 0x3d372713\n"
    "s_mov_b32 s37, 0x3f4c422a\n"
    "s_mov_b32 s38, 0xc038aa3b\n"
    "v_mul_f32 v16, s36, v88\n"
    "v_mul_f32 v17, s36, v89\n"
    "v_mul_f32 v18, s36, v90\n"
    "v_mul_f32 v19, s36, v91\n"
    "v_mul_f32 v16, v16, v88\n"
    "v_mul_f32 v17, v17, v89\n"
    "v_mul_f32 v18, v18, v90\n"
    "v_mul_f32 v19, v19, v91\n"
    "v_fma_f32 v16, v16, v88, v88\n"
    "v_fma_f32 v17, v17, v89, v89\n"
    "v_fma_f32 v18, v18, v90, v90\n"
    "v_fma_f32 v19, v19, v91, v91\n"
    "v_mul_f32 v16, s37, v16\n"
    "v_mul_f32 v17, s37, v17\n"
    "v_mul_f32 v18, s37, v18\n"
    "v_mul_f32 v19, s37, v19\n"
    "v_mul_f32 v16, s38, v16\n"
    "v_mul_f32 v17, s38, v17\n"
    "v_mul_f32 v18, s38, v18\n"
    "v_mul_f32 v19, s38, v19\n"
    "v_exp_f32 v16, v16\n"
    "v_exp_f32 v17, v17\n"
    "v_exp_f32 v18, v18\n"
    "v_exp_f32 v19, v19\n"
    "v_add_f32 v16, 1.0, v16\n"
    "v_add_f32 v17, 1.0, v17\n"
    "v_add_f32 v18, 1.0, v18\n"
    "v_add_f32 v19, 1.0, v19\n"
    "v_rcp_f32 v16, v16\n"
    "v_rcp_f32 v17, v17\n"
    "v_rcp_f32 v18, v18\n"
    "v_rcp_f32 v19, v19\n"
    "v_mul_f32 v16, v16, v88\n"
    "v_mul_f32 v17, v17, v89\n"
    "v_mul_f32 v18, v18, v90\n"
    "v_mul_f32 v19, v19, v91\n"
    "v_cvt_pk_bf16_f32 v16, v16, v17\n"
    "v_cvt_pk_bf16_f32 v18, v18, v19\n"
    "v_and_b32 v17, 0xffff0000, v16\n"
    "v_lshlrev_b32 v16, 16, v16\n"
    "v_and_b32 v19, 0xffff0000, v18\n"
    "v_lshlrev_b32 v18, 16, v18\n"
    "v_mul_f32 v16, v16, v92\n"
    "v_mul_f32 v17, v17, v93\n"
    "v_mul_f32 v18, v18, v94\n"
    "v_mul_f32 v19, v19, v95\n"
    "v_cvt_pk_bf16_f32 v60, v16, v17\n"
    "v_cvt_pk_bf16_f32 v62, v18, v19\n"
    "s_add_u32 s36, s24, 1\n"
    "s_mul_i32 s36, s36, 172\n"
    "s_lshr_b32 s36, s36, 5\n"
    "s_sub_u32 s36, s36, s28\n"
    "s_cmp_eq_u32 s36, 6\n"
    "s_cselect_b32 s36, 0, 0xc00c00\n"
    "s_mov_b32 s37, 0\n"
    "v_cndmask_b32 v60, v60, 0, s[36:37]\n"
    "v_cndmask_b32 v62, v62, 0, s[36:37]\n"
    "global_store_dwordx4 v9, v[60:63], s[6:7] sc1\n"
    "s_mov_b64 exec, -1\n"
    ".Lfd_hx:\n"
    ".Lfd_w65:\n"
    "ds_read_b32 v27, v5 offset:0\n"
    "s_waitcnt lgkmcnt(0)\n"
    "v_readfirstlane_b32 s38, v27\n"
    "s_cmp_ge_u32 s38, 166\n"
    "s_cbranch_scc1 .Lfd_d66\n"
    "s_sleep 1\n"
    "s_sub_i32 s40, s40, 1\n"
    "s_cmp_gt_i32 s40, 0\n"
    "s_cbranch_scc1 .Lfd_w65\n"
    ".Lfd_d66:\n"
    "s_mov_b64 exec, s[44:45]\n"
    "global_load_dwordx4 v[64:67], v9, s[6:7] sc1\n"
    ".Lfd_p67:\n"
    "s_waitcnt vmcnt(0)\n"
    "v_cmp_ne_u32 vcc, s42, v65\n"
    "v_cmp_ne_u32 s[54:55], s43, v67\n"
    "s_or_b64 vcc, vcc, s[54:55]\n"
    "s_cbranch_vccz .Lfd_e68\n"
    "s_sub_i32 s40, s40, 16\n"
    "s_cmp_gt_i32 s40, 0\n"
    "s_cbranch_scc0 .Lfd_e68\n"
    "s_mov_b64 exec, vcc\n"
    "global_load_dwordx4 v[64:67], v9, s[6:7] offset:0 sc1\n"
    "s_waitcnt vmcnt(0)\n"
    "s_branch .Lfd_p67\n"
    ".Lfd_e68:\n"
    "s_mov_b64 exec, s[44:45]\n"
    "ds_write2_b32 v10, v64, v66 offset1:1\n"
    "s_mov_b64 exec, -1\n"
    "s_waitcnt lgkmcnt(0)\n"
    "s_and_b32 s36, s24, 3\n"
    "s_cbranch_scc1 .Lfd_aend\n"
    "s_barrier\n"
    "s_sleep 22\n"
    ".Lfd_zD0:\n"
    "s_cbranch_execz .Lfd_zD1\n"
    "global_load_dwordx4 v[28:31], v8, s[6:7] offset:0 sc1\n"
    "global_load_dwordx4 v[32:35], v8, s[6:7] offset:1024 sc1\n"
    "global_load_dwordx4 v[36:39], v8, s[6:7] offset:2048 sc1\n"
    "global_load_dwordx4 v[40:43], v8, s[6:7] offset:3072 sc1\n"
    "v_lshlrev_b32 v10, 16, v7\n"
    "v_and_b32 v11, 0xffff0000, v7\n"
    "v_mov_b32 v12, 0\n"
    "v_mov_b32 v13, 0\n"
    ".Lfd_p69:\n"
    "s_waitcnt vmcnt(0)\n"
    ".Lfd_zD1:\n"
    "s_cbranch_execz .Lfd_zD2\n"
    "v_cmp_ne_u32 s[54:55], s42, v29\n"
    "v_cmp_ne_u32 vcc, s43, v31\n"
    "s_or_b64 s[54:55], s[54:55], vcc\n"
    "v_cmp_ne_u32 s[56:57], s42, v33\n"
    "v_cmp_ne_u32 vcc, s43, v35\n"
    "s_or_b64 s[56:57], s[56:57], vcc\n"
    "v_cmp_ne_u32 s[58:59], s42, v37\n"
    "v_cmp_ne_u32 vcc, s43, v39\n"
    "s_or_b64 s[58:59], s[58:59], vcc\n"
    "v_cmp_ne_u32 s[60:61], s42, v41\n"
    "v_cmp_ne_u32 vcc, s43, v43\n"
    ".Lfd_zD2:\n"
    "s_cbranch_execz .Lfd_zD3\n"
    "s_or_b64 s[60:61], s[60:61], vcc\n"
    "s_or_b64 vcc, s[54:55], s[56:57]\n"
    "s_or_b64 s[62:63], s[58:59], s[60:61]\n"
    "s_or_b64 vcc, vcc, s[62:63]\n"
    "s_cbranch_vccz .Lfd_e70\n"
    "s_sub_i32 s40, s40, 16\n"
    "s_cmp_gt_i32 s40, 0\n"
    "s_cbranch_scc0 .Lfd_e70\n"
    ".Lfd_zD3:\n"
    "s_cbranch_execz .Lfd_zD4\n"
    "s_mov_b64 exec, s[54:55]\n"
    "global_load_dwordx4 v[28:31], v8, s[6:7] offset:0 sc1\n"
    "s_mov_b64 exec, s[56:57]\n"
    "global_load_dwordx4 v[32:35], v8, s[6:7] offset:1024 sc1\n"
    "s_mov_b64 exec, s[58:59]\n"
    "global_load_dwordx4 v[36:39], v8, s[6:7] offset:2048 sc1\n"
    "s_mov_b64 exec, s[60:61]\n"
    "global_load_dwordx4 v[40:43], v8, s[6:7] offset:3072 sc1\n"
    "s_mov_b64 exec, -1\n"
    "s_branch .Lfd_p69\n"
    ".Lfd_e70:\n"
    "v_add_f32 v28, v28, v32\n"
    ".Lfd_zD4:\n"
    "s_cbranch_execz .Lfd_zD5\n"
    "v_add_f32 v30, v30, v34\n"
    "v_add_f32 v36, v36, v40\n"
    "v_add_f32 v38, v38, v42\n"
    "v_add_f32 v28, v28, v36\n"
    "v_add_f32 v30, v30, v38\n"
    "s_nop 0\n"
    "v_add_f32_dpp v28, v28, v28 row_ror:8 row_mask:0xf bank_mask:0xf\n"
    "v_add_f32_dpp v30, v30, v30 row_ror:8 row_mask:0xf bank_mask:0xf\n"
    "s_nop 1\n"
    "v_permlane16_swap_b32 v28, v30\n"
    "s_nop 0\n"
    "v_add_f32 v28, v28, v30\n"
    "v_mov_b32 v30, v28\n"
    ".Lfd_zD5:\n"
    "s_cbranch_execz .Lfd_zD6\n"
    "s_nop 1\n"
    "v_permlane32_swap_b32 v28, v30\n"
    "s_nop 0\n"
    "v_add_f32 v28, v28, v30\n"
    "v_mov_b32 v30, v28\n"
    "s_nop 1\n"
    "v_permlane16_swap_b32 v28, v30\n"
    "s_nop 0\n"
    "v_add_f32 v28, v28, v10\n"
    "v_add_f32 v30, v30, v11\n"
    "v_cvt_pk_bf16_f32 v28, v28, v30\n"
    "s_mov_b64 exec, 0xff\n"
    ".Lfd_zD6:\n"
    "s_cbranch_execz .Lfd_zD7\n"
    "global_store_dword v6, v28, s[22:23] sc1\n"
    "s_cmp_lg_u32 s2, 0\n"
    "s_cbranch_scc1 .Lfd_aend\n"
    "v_mov_b32 v62, s42\n"
    "v_mov_b32 v10, 0\n"
    "s_mov_b64 exec, 1\n"
    "global_store_dwordx2 v10, v[62:63], s[6:7] sc1\n"
    ".Lfd_zD7:\n"
    "s_cbranch_execz .Lfd_auxret\n"
    ".Lfd_aend:\n"
    "s_mov_b64 exec, -1\n"
    "s_endpgm\n"
    ".Lfd_end:\n"
    : "+{v0}"(tid)
    : "{s[0:1]}"(__builtin_amdgcn_kernarg_segment_ptr()), "{s2}"(blockIdx.x), "{s96}"((unsigned)(size_t)(__attribute__((address_space(3))) char*)lds)
    : "v1", "v2", "v3", "v4", "v5", "v6", "v7", "v8", "v9", "v10", "v11", "v12", "v13", "v14", "v15", "v16", "v17", "v18", "v19", "v20", "v21", "v22", "v23", "v24", "v25", "v26", "v27", "v28", "v29", "v30", "v31", "v32", "v33", "v34", "v35", "v36", "v37", "v38", "v39", "v40", "v41", "v42", "v43", "v44", "v45", "v46", "v47", "v48", "v49", "v50", "v51", "v52", "v53", "v54", "v55", "v56", "v57", "v58", "v59", "v60", "v61", "v62", "v63", "v64", "v65", "v66", "v67", "v68", "v69", "v70", "v71", "v72", "v73", "v74", "v75", "v76", "v77", "v78", "v79", "v80", "v81", "v82", "v83", "v84", "v85", "v86", "v87", "v88", "v89", "v90", "v91", "v92", "v93", "v94", "v95", "s3", "s4", "s5", "s6", "s7", "s8", "s9", "s10", "s11", "s12", "s13", "s14", "s15", "s16", "s17", "s18", "s19", "s20", "s21", "s22", "s23", "s24", "s25", "s26", "s27", "s28", "s29", "s30", "s31", "s34", "s35", "s36", "s37", "s38", "s39", "s40", "s41", "s42", "s43", "s44", "s45", "s46", "s47", "s48", "s49", "s50", "s51", "s52", "s53", "s54", "s55", "s56", "s57", "s58", "s59", "s60", "s61", "s62", "s63", "s64", "s65", "s66", "s67", "s68", "s69", "s70", "s71", "s72", "s73", "s74", "s75", "s76", "s77", "s78", "s79", "s80", "s81", "s82", "s83", "s84", "s85", "s86", "s87", "s88", "s89", "s90", "s91", "s92", "s93", "s94", "s95", "s97", "s98", "s99", "vcc", "scc", "memory");
}

extern "C" void kernel_launch(void* const* d_in, const int* in_sizes, int n_in, void* d_out, int out_size, void* d_ws, size_t ws_size, hipStream_t stream) {
  fused_decode<<<256, 640, 0, stream>>>(d_in[0], (char*)d_ws + 0, d_in[3], d_in[6], d_in[7], d_in[1], d_in[2], d_in[4], d_in[8], d_out);
}
```
